```python
import torch

def _softmax(x, axis=-1):
    return torch.softmax(x, dim=axis)
def _gelu(x):
    return torch.nn.functional.gelu(x, approximate="tanh")
def _silu(x):
    return torch.nn.functional.silu(x)
def _sigmoid(x):
    return torch.sigmoid(x)
def _layernorm(x, axis=-1, eps=1e-5):
    m = x.mean(dim=axis, keepdim=True)
    v = x.var(dim=axis, keepdim=True, unbiased=False)
    return (x - m) / torch.sqrt(v + eps)
def _scatter_add(tgt, idx, src, axis=0):
    return tgt.clone().index_add_(axis, idx, src)
def _segment_sum(src, seg, n):
    out = torch.zeros((n,) + src.shape[1:], dtype=src.dtype)
    return out.index_add_(0, seg, src)
def _pad(x, pw):
    flat = tuple(v for pair in reversed(pw) for v in pair)
    return torch.nn.functional.pad(x, flat)

kernel_name = 'standalone__paged_block_table_kv_radixattention__decode'

def setup_inputs():

    _w = (lambda a: (lambda t: t.to(torch.bfloat16) if t.is_floating_point() else t)(torch.as_tensor(a)))
    B, H, Hkv, Dh = 1, 32, 4, 128
    Lk = 65536
    bs = 16
    nb = max(2, Lk // bs)
    P = max(1, nb // 2)
    n_phys = P + B * (nb - P)
    shared = torch.arange(P, dtype=torch.int64)
    priv = P + torch.arange(B * (nb - P), dtype=torch.int64).reshape(B, nb - P)
    bt = torch.cat([torch.tile(shared[None, :], (B, 1)), priv], dim=1)
    ctx = nb * bs - (torch.arange(1, B + 1, dtype=torch.int64) * 2654435761) % max(1, (nb * bs - P * bs) // 2)
    return {
        "q": _w((lambda *s: torch.randn(*s, dtype=torch.bfloat16) * 0.8)(B, H, Dh)),
        "k_cache": _w((lambda *s: torch.randn(*s, dtype=torch.bfloat16) * 0.8)(n_phys, bs, Hkv, Dh)),
        "v_cache": _w((lambda *s: torch.randn(*s, dtype=torch.bfloat16) * 0.05)(n_phys, bs, Hkv, Dh)),
        "block_table": _w(bt.to(torch.int64)),
        "ctx_lens": _w(torch.as_tensor(ctx).to(torch.int64)),
    }

def reference(q, k_cache, v_cache, block_table, ctx_lens):
    B, H, Dh = q.shape
    n_phys, bs, Hkv, _ = k_cache.shape
    nb = block_table.shape[1]
    Lk = nb * bs
    rep = H // Hkv
    k = k_cache[block_table].reshape(B, Lk, Hkv, Dh)
    v = v_cache[block_table].reshape(B, Lk, Hkv, Dh)
    k = (lambda x, n, axis: torch.repeat_interleave(x, n, dim=axis))(k, rep, axis=2)
    v = (lambda x, n, axis: torch.repeat_interleave(x, n, dim=axis))(v, rep, axis=2)
    scores = torch.einsum("bhd,bkhd->bhk", q, k) * (Dh ** -0.5)
    pos = torch.arange(Lk)
    valid = pos[None, :] < ctx_lens[:, None]
    scores = torch.where(valid[:, None, :], scores, -1e9)
    p = _softmax(scores, axis=-1)
    return torch.einsum("bhk,bkhd->bhd", p, v)
```

```cpp
#include <hip/hip_runtime.h>
#include <hip/hip_bf16.h>
#include <stdint.h>

#define KTRICK 1
#define KV_AUX 2

namespace {

constexpr int H = 32, HKV = 4, D = 128, BS = 16, NB = 4096, G = H / HKV;
constexpr int LK = NB * BS;
constexpr int NSPLIT = 64, NWAVE = 8, NTHR = NWAVE * 64;
constexpr int UPW = NB / (NSPLIT * NWAVE);
static_assert(UPW == 8, "straight-line body is written for 8 pages per wave");
constexpr int PAGE_BYTES = BS * HKV * D * 2;
constexpr float SCALE = 0.08838834764831845f;
constexpr float C2 = SCALE * 1.4426950408889634f;

typedef unsigned v4u __attribute__((ext_vector_type(4)));
typedef float f32x4 __attribute__((ext_vector_type(4)));
typedef __bf16 bf16x8 __attribute__((ext_vector_type(8)));
typedef __bf16 bf16x2 __attribute__((ext_vector_type(2)));
typedef float f32x2 __attribute__((ext_vector_type(2)));

__device__ __forceinline__ f32x4 mfma(v4u a, v4u b, f32x4 c) {
  return __builtin_amdgcn_mfma_f32_16x16x32_bf16(__builtin_bit_cast(bf16x8, a), __builtin_bit_cast(bf16x8, b), c, 0, 0, 0);
}
__device__ __forceinline__ unsigned cvtpk(float lo, float hi) {
  f32x2 v = {lo, hi};
  bf16x2 r = __builtin_convertvector(v, bf16x2);
  return __builtin_bit_cast(unsigned, r);
}
template <int CTRL> __device__ __forceinline__ float dpp(float x) {
  return __builtin_bit_cast(float, __builtin_amdgcn_mov_dpp(__builtin_bit_cast(int, x), CTRL, 0xf, 0xf, true));
}
__device__ __forceinline__ float xg_max(float x) {
  auto s = __builtin_amdgcn_permlane16_swap(__float_as_uint(x), __float_as_uint(x), false, false);
  x = fmaxf(__uint_as_float(s[0]), __uint_as_float(s[1]));
  auto t = __builtin_amdgcn_permlane32_swap(__float_as_uint(x), __float_as_uint(x), false, false);
  return fmaxf(__uint_as_float(t[0]), __uint_as_float(t[1]));
}
__device__ __forceinline__ float xg_sum(float x) {
  auto s = __builtin_amdgcn_permlane16_swap(__float_as_uint(x), __float_as_uint(x), false, false);
  x = __uint_as_float(s[0]) + __uint_as_float(s[1]);
  auto t = __builtin_amdgcn_permlane32_swap(__float_as_uint(x), __float_as_uint(x), false, false);
  return __uint_as_float(t[0]) + __uint_as_float(t[1]);
}

struct Page { v4u r[4]; };

__device__ __forceinline__ Page load_k(const char* kbase, int phys, bool valid, int voff) {
  __amdgpu_buffer_rsrc_t rs = __builtin_amdgcn_make_buffer_rsrc((void*)(kbase + (size_t)(unsigned)phys * PAGE_BYTES), 0,
                                                               valid ? PAGE_BYTES : 0, 0x00020000);
  Page p;
  p.r[0] = __builtin_amdgcn_raw_buffer_load_b128(rs, voff, 0, KV_AUX);
  p.r[1] = __builtin_amdgcn_raw_buffer_load_b128(rs, voff + 128, 0, KV_AUX);
  p.r[2] = __builtin_amdgcn_raw_buffer_load_b128(rs, voff, 8192, KV_AUX);
  p.r[3] = __builtin_amdgcn_raw_buffer_load_b128(rs, voff + 128, 8192, KV_AUX);
  return p;
}
__device__ __forceinline__ Page load_v(const char* vbase, int phys, bool valid, int voff) {
  __amdgpu_buffer_rsrc_t rs = __builtin_amdgcn_make_buffer_rsrc((void*)(vbase + (size_t)(unsigned)phys * PAGE_BYTES), 0,
                                                               valid ? PAGE_BYTES : 0, 0x00020000);
  Page p;
  p.r[0] = __builtin_amdgcn_raw_buffer_load_b128(rs, voff, 0, KV_AUX);
  p.r[1] = __builtin_amdgcn_raw_buffer_load_b128(rs, voff + 1024, 0, KV_AUX);
  p.r[2] = __builtin_amdgcn_raw_buffer_load_b128(rs, voff + 2048, 0, KV_AUX);
  p.r[3] = __builtin_amdgcn_raw_buffer_load_b128(rs, voff + 3072, 0, KV_AUX);
  return p;
}

struct QFrag { v4u q[2]; };
__device__ __forceinline__ f32x4 qk_page(const Page& k, const QFrag& q) {
  const f32x4 z = {0.f, 0.f, 0.f, 0.f};
  f32x4 d0 = mfma(k.r[0], q.q[0], z);
  f32x4 d1 = mfma(k.r[2], q.q[0], z);
  d0 = mfma(k.r[1], q.q[1], d0);
  d1 = mfma(k.r[3], q.q[1], d1);
  f32x4 x;
#pragma unroll
  for (int r = 0; r < 4; ++r) {
    auto s = __builtin_amdgcn_permlane32_swap(__float_as_uint(d0[r]), __float_as_uint(d1[r]), false, false);
    x[r] = __uint_as_float(s[0]) + dpp<0x128>(__uint_as_float(s[1]));
  }
  return x;
}

struct State { float m, l; f32x4 acc[8]; };

template <bool FIRST>
__device__ __forceinline__ v4u softmax_step(State& st, f32x4 xa, f32x4 xb, int lima, int limb) {
#pragma unroll
  for (int r = 0; r < 4; ++r) {
    xa[r] = (r < lima) ? xa[r] : -INFINITY;
    xb[r] = (r < limb) ? xb[r] : -INFINITY;
  }
  float mx = fmaxf(fmaxf(fmaxf(xa[0], xa[1]), fmaxf(xa[2], xa[3])), fmaxf(fmaxf(xb[0], xb[1]), fmaxf(xb[2], xb[3])));
  mx = xg_max(mx);
  float mn = FIRST ? mx : fmaxf(st.m, mx);
  const float mc = mn * C2;
  float pa[4], pb[4];
#pragma unroll
  for (int r = 0; r < 4; ++r) {
    pa[r] = __builtin_amdgcn_exp2f(fmaf(xa[r], C2, -mc));
    pb[r] = __builtin_amdgcn_exp2f(fmaf(xb[r], C2, -mc));
  }
  const float ps = ((pa[0] + pa[1]) + (pa[2] + pa[3])) + ((pb[0] + pb[1]) + (pb[2] + pb[3]));
  if (FIRST) {
    st.l = ps;
  } else {
    const float alpha = __builtin_amdgcn_exp2f(fmaf(st.m, C2, -mc));
    st.l = fmaf(st.l, alpha, ps);
#pragma unroll
    for (int m = 0; m < 8; ++m) st.acc[m] *= alpha;
  }
  st.m = mn;
  v4u p;
  p[0] = cvtpk(pa[0], pa[1]);
  p[1] = cvtpk(pa[2], pa[3]);
  p[2] = cvtpk(pb[0], pb[1]);
  p[3] = cvtpk(pb[2], pb[3]);
  return p;
}

template <bool FIRST>
__device__ __forceinline__ void pv_step(State& st, const Page& va, const Page& vb, v4u p) {
#pragma unroll
  for (int m = 0; m < 8; ++m) {
    const int w = m >> 1;
    const unsigned sel = (m & 1) ? 0x07060302u : 0x05040100u;
    v4u f;
    f[0] = __builtin_amdgcn_perm(va.r[1][w], va.r[0][w], sel);
    f[1] = __builtin_amdgcn_perm(va.r[3][w], va.r[2][w], sel);
    f[2] = __builtin_amdgcn_perm(vb.r[1][w], vb.r[0][w], sel);
    f[3] = __builtin_amdgcn_perm(vb.r[3][w], vb.r[2][w], sel);
    if (FIRST) {
      const f32x4 z = {0.f, 0.f, 0.f, 0.f};
      st.acc[m] = mfma(f, p, z);
    } else {
      st.acc[m] = mfma(f, p, st.acc[m]);
    }
  }
}

constexpr int LDS_ROW = D + 4;

#define SB() do { asm volatile("" ::: "memory"); __builtin_amdgcn_sched_barrier(0); } while (0)

__global__ void __launch_bounds__(NTHR, 2)
attn_split(const __hip_bfloat16* q, const char* kc, const char* vc,
           const int* __restrict__ bt, const int* __restrict__ ctx_lens, float* __restrict__ po, float* __restrict__ pml) {
  __shared__ __attribute__((aligned(16))) float lds[NWAVE * G * LDS_ROW];
  const int tid = threadIdx.x, lane = tid & 63;
  const int w = __builtin_amdgcn_readfirstlane(tid >> 6);
  const int kvh = blockIdx.x & 3, s = blockIdx.x >> 2;
  const int n = lane & 15, g = lane >> 4;

  int phys[UPW];
#pragma unroll
  for (int i = 0; i < UPW; ++i) phys[i] = bt[(i * NWAVE + w) * NSPLIT + s];
  int ctx = ctx_lens[0];
  ctx = ctx < 0 ? 0 : (ctx > LK ? LK : ctx);

  QFrag qf;
  const char* qrow = (const char*)q + (size_t)(kvh * G + (n & 7)) * (D * 2);
  qf.q[0] = *(const v4u*)(qrow + (n >> 3) * 64 + g * 16);
  qf.q[1] = *(const v4u*)(qrow + 128 + (n >> 3) * 64 + g * 16);
  const int koff = (n & 7) * 1024 + (n >> 3) * 64 + g * 16;
  const int voff = g * 4096 + n * 16;
  const char* kb = kc + kvh * 256;
  const char* vb = vc + kvh * 256;

  int rem[UPW];
#pragma unroll
  for (int i = 0; i < UPW; ++i) rem[i] = ctx - ((i * NWAVE + w) * NSPLIT + s) * BS;
  const int g4 = 4 * g;

  State st;
  st.m = -INFINITY; st.l = 0.f;
#pragma unroll
  for (int m = 0; m < 8; ++m) st.acc[m] = f32x4{0.f, 0.f, 0.f, 0.f};

  asm volatile("" :: "s"(phys[0]), "s"(phys[1]), "s"(phys[2]), "s"(phys[3]), "s"(phys[4]), "s"(phys[5]), "s"(phys[6]), "s"(phys[7]), "s"(ctx));
  SB();
  Page K0 = load_k(kb, phys[0], rem[0] > 0, koff);
  Page K1 = load_k(kb, phys[1], rem[1] > 0, koff);
  Page V0 = load_v(vb, phys[0], rem[0] > 0, voff);
  Page V1 = load_v(vb, phys[1], rem[1] > 0, voff);
  Page K2 = load_k(kb, phys[2], rem[2] > 0, koff);
  Page K3 = load_k(kb, phys[3], rem[3] > 0, koff);
  Page V2 = load_v(vb, phys[2], rem[2] > 0, voff);
  Page V3 = load_v(vb, phys[3], rem[3] > 0, voff);
  Page K4 = load_k(kb, phys[4], rem[4] > 0, koff);
  Page K5 = load_k(kb, phys[5], rem[5] > 0, koff);
  SB();
  if (rem[0] > 0) {
    f32x4 xa = qk_page(K0, qf), xb = qk_page(K1, qf);
    SB();
    Page V4 = load_v(vb, phys[4], rem[4] > 0, voff);
    Page V5 = load_v(vb, phys[5], rem[5] > 0, voff);
    SB();
    v4u p = softmax_step<true>(st, xa, xb, rem[0] - g4, rem[1] - g4);
    pv_step<true>(st, V0, V1, p);
    SB();
    Page K6 = load_k(kb, phys[6], rem[6] > 0, koff);
    Page K7 = load_k(kb, phys[7], rem[7] > 0, koff);
    SB();
    if (rem[2] > 0) {
      xa = qk_page(K2, qf); xb = qk_page(K3, qf);
      SB();
      Page V6 = load_v(vb, phys[6], rem[6] > 0, voff);
      Page V7 = load_v(vb, phys[7], rem[7] > 0, voff);
      SB();
      p = softmax_step<false>(st, xa, xb, rem[2] - g4, rem[3] - g4);
      pv_step<false>(st, V2, V3, p);
      SB();
      if (rem[4] > 0) {
        xa = qk_page(K4, qf); xb = qk_page(K5, qf);
        p = softmax_step<false>(st, xa, xb, rem[4] - g4, rem[5] - g4);
        pv_step<false>(st, V4, V5, p);
        SB();
        if (rem[6] > 0) {
          xa = qk_page(K6, qf); xb = qk_page(K7, qf);
          p = softmax_step<false>(st, xa, xb, rem[6] - g4, rem[7] - g4);
          pv_step<false>(st, V6, V7, p);
        }
      }
    }
  }
  const float lsum = xg_sum(st.l);
  if (n < G) {
    float* rec = lds + (w * G + n) * LDS_ROW;
#pragma unroll
    for (int r = 0; r < 4; ++r) {
      *(f32x4*)(rec + 32 * g + 8 * r) = f32x4{st.acc[0][r], st.acc[1][r], st.acc[2][r], st.acc[3][r]};
      *(f32x4*)(rec + 32 * g + 8 * r + 4) = f32x4{st.acc[4][r], st.acc[5][r], st.acc[6][r], st.acc[7][r]};
    }
    if (g == 0) { rec[D] = st.m; rec[D + 1] = lsum; }
  }
  __syncthreads();
  {
    const int hh = w;
    float mw[NWAVE], lw[NWAVE];
    float M = -INFINITY;
#pragma unroll
    for (int i = 0; i < NWAVE; ++i) {
      const f32x2 ml = *(const f32x2*)(lds + (i * G + hh) * LDS_ROW + D);
      mw[i] = ml[0]; lw[i] = ml[1];
      M = fmaxf(M, mw[i]);
    }
    const float Mc = (M == -INFINITY) ? 0.f : M * C2;
    float L = 0.f, o0 = 0.f, o1 = 0.f;
#pragma unroll
    for (int i = 0; i < NWAVE; ++i) {
      const float e = __builtin_amdgcn_exp2f(fmaf(mw[i], C2, -Mc));
      const f32x2 a = *(const f32x2*)(lds + (i * G + hh) * LDS_ROW + 2 * lane);
      L = fmaf(e, lw[i], L); o0 = fmaf(e, a[0], o0); o1 = fmaf(e, a[1], o1);
    }
    const int slot = (kvh * G + hh) * NSPLIT + s;
    *(f32x2*)(po + (size_t)slot * D + 2 * lane) = f32x2{o0, o1};
    if (lane == 0) *(f32x2*)(pml + 2 * slot) = f32x2{M, L};
  }
}

__global__ void __launch_bounds__(256)
attn_combine(const float* __restrict__ po, const float* __restrict__ pml, const __hip_bfloat16* __restrict__ vcache,
             const int* __restrict__ bt, const int* __restrict__ ctx_lens, __hip_bfloat16* __restrict__ out) {
  __shared__ __attribute__((aligned(16))) float red[32 * 36];
  const int tid = threadIdx.x, lane = tid & 63;
  const int h = blockIdx.x >> 2, ds = blockIdx.x & 3;
  const int c = tid & 7, sg = tid >> 3;
  const f32x4 a0 = *(const f32x4*)(po + (size_t)(h * NSPLIT + sg) * D + ds * 32 + c * 4);
  const f32x4 a1 = *(const f32x4*)(po + (size_t)(h * NSPLIT + sg + 32) * D + ds * 32 + c * 4);
  const f32x2 ml = *(const f32x2*)(pml + 2 * (h * NSPLIT + lane));
  int ctx = ctx_lens[0];
  ctx = ctx < 0 ? 0 : (ctx > LK ? LK : ctx);
  float M = ml[0];
#pragma unroll
  for (int o = 1; o < 64; o <<= 1) M = fmaxf(M, __shfl_xor(M, o));
  const float Mc = (M == -INFINITY) ? 0.f : M * C2;
  const float e = __builtin_amdgcn_exp2f(fmaf(ml[0], C2, -Mc));
  float L = e * ml[1];
#pragma unroll
  for (int o = 1; o < 64; o <<= 1) L += __shfl_xor(L, o);
  const float e0 = __shfl(e, sg), e1 = __shfl(e, sg + 32);
  f32x4 o4 = a0 * e0 + a1 * e1;
  const float Mnat = M * SCALE;
  float wv = 1.f, wm = 0.f;
  const bool slow = (ctx < LK) && !(Mnat > -1.0e9f + 512.f);
  if (slow) {
    const float Mall = fmaxf(Mnat, -1.0e9f);
    wv = (M == -INFINITY) ? 0.f : __expf(Mnat - Mall);
    wm = __expf(-1.0e9f - Mall);
    f32x4 sv = {0.f, 0.f, 0.f, 0.f};
    const int kvh = h / G;
    for (int t = ctx + sg; t < LK; t += 32) {
      const __hip_bfloat16* vp = vcache + ((size_t)bt[t / BS] * BS + (t % BS)) * (HKV * D) + kvh * D + ds * 32 + c * 4;
#pragma unroll
      for (int j = 0; j < 4; ++j) sv[j] += __bfloat162float(vp[j]);
    }
    o4 = o4 * wv + sv * wm;
    L = L * wv + wm * (float)(LK - ctx);
  }
  *(f32x4*)(red + sg * 36 + c * 4) = o4;
  __syncthreads();
  if (tid < 32) {
    float acc = 0.f;
#pragma unroll
    for (int i = 0; i < 32; ++i) acc += red[i * 36 + tid];
    out[h * D + ds * 32 + tid] = __float2bfloat16(acc / L);
  }
}

}

extern "C" void kernel_launch(void* const* d_in, const int* in_sizes, int n_in, void* d_out, int out_size, void* d_ws,
                              size_t ws_size, hipStream_t stream) {
  const __hip_bfloat16* q = (const __hip_bfloat16*)d_in[0];
  const char* kc = (const char*)d_in[1];
  const char* vc = (const char*)d_in[2];
  const int* bt = (const int*)d_in[3];
  const int* ctx = (const int*)d_in[4];
  float* po = (float*)d_ws;
  float* pml = po + (size_t)H * NSPLIT * D;
  attn_split<<<HKV * NSPLIT, NTHR, 0, stream>>>(q, kc, vc, bt, ctx, po, pml);
  attn_combine<<<H * 4, 256, 0, stream>>>(po, pml, (const __hip_bfloat16*)vc, bt, ctx, (__hip_bfloat16*)d_out);
}
```

```cpp
#include <hip/hip_runtime.h>
#include <hip/hip_bf16.h>
#include <stdint.h>

#define KTRICK 1
#define KV_AUX 2

#define TS(i)
namespace {

constexpr int H = 32, HKV = 4, D = 128, BS = 16, NB = 4096, G = H / HKV;
constexpr int LK = NB * BS;
constexpr int NSPLIT = 64, NWAVE = 8, NTHR = NWAVE * 64;
constexpr int UPW = NB / (NSPLIT * NWAVE);
static_assert(UPW == 8, "straight-line body is written for 8 pages per wave");
constexpr int PAGE_BYTES = BS * HKV * D * 2;
constexpr float SCALE = 0.08838834764831845f;
constexpr float C2 = SCALE * 1.4426950408889634f;

typedef unsigned v4u __attribute__((ext_vector_type(4)));
typedef float f32x4 __attribute__((ext_vector_type(4)));
typedef __bf16 bf16x8 __attribute__((ext_vector_type(8)));
typedef __bf16 bf16x2 __attribute__((ext_vector_type(2)));
typedef float f32x2 __attribute__((ext_vector_type(2)));

__device__ __forceinline__ f32x4 mfma(v4u a, v4u b, f32x4 c) {
  return __builtin_amdgcn_mfma_f32_16x16x32_bf16(__builtin_bit_cast(bf16x8, a), __builtin_bit_cast(bf16x8, b), c, 0, 0, 0);
}
__device__ __forceinline__ unsigned cvtpk(float lo, float hi) {
  f32x2 v = {lo, hi};
  bf16x2 r = __builtin_convertvector(v, bf16x2);
  return __builtin_bit_cast(unsigned, r);
}
template <int CTRL> __device__ __forceinline__ float dpp(float x) {
  return __builtin_bit_cast(float, __builtin_amdgcn_mov_dpp(__builtin_bit_cast(int, x), CTRL, 0xf, 0xf, true));
}
__device__ __forceinline__ float xg_max(float x) {
  auto s = __builtin_amdgcn_permlane16_swap(__float_as_uint(x), __float_as_uint(x), false, false);
  x = fmaxf(__uint_as_float(s[0]), __uint_as_float(s[1]));
  auto t = __builtin_amdgcn_permlane32_swap(__float_as_uint(x), __float_as_uint(x), false, false);
  return fmaxf(__uint_as_float(t[0]), __uint_as_float(t[1]));
}
__device__ __forceinline__ float xg_sum(float x) {
  auto s = __builtin_amdgcn_permlane16_swap(__float_as_uint(x), __float_as_uint(x), false, false);
  x = __uint_as_float(s[0]) + __uint_as_float(s[1]);
  auto t = __builtin_amdgcn_permlane32_swap(__float_as_uint(x), __float_as_uint(x), false, false);
  return __uint_as_float(t[0]) + __uint_as_float(t[1]);
}

struct Page { v4u r[4]; };

__device__ __forceinline__ Page load_k(const char* kbase, int phys, bool valid, int voff) {
  __amdgpu_buffer_rsrc_t rs = __builtin_amdgcn_make_buffer_rsrc((void*)(kbase + (size_t)(unsigned)phys * PAGE_BYTES), 0,
                                                               valid ? PAGE_BYTES : 0, 0x00020000);
  Page p;
  p.r[0] = __builtin_amdgcn_raw_buffer_load_b128(rs, voff, 0, KV_AUX);
  p.r[1] = __builtin_amdgcn_raw_buffer_load_b128(rs, voff + 128, 0, KV_AUX);
  p.r[2] = __builtin_amdgcn_raw_buffer_load_b128(rs, voff, 8192, KV_AUX);
  p.r[3] = __builtin_amdgcn_raw_buffer_load_b128(rs, voff + 128, 8192, KV_AUX);
  return p;
}
__device__ __forceinline__ Page load_v(const char* vbase, int phys, bool valid, int voff) {
  __amdgpu_buffer_rsrc_t rs = __builtin_amdgcn_make_buffer_rsrc((void*)(vbase + (size_t)(unsigned)phys * PAGE_BYTES), 0,
                                                               valid ? PAGE_BYTES : 0, 0x00020000);
  Page p;
  p.r[0] = __builtin_amdgcn_raw_buffer_load_b128(rs, voff, 0, KV_AUX);
  p.r[1] = __builtin_amdgcn_raw_buffer_load_b128(rs, voff + 1024, 0, KV_AUX);
  p.r[2] = __builtin_amdgcn_raw_buffer_load_b128(rs, voff + 2048, 0, KV_AUX);
  p.r[3] = __builtin_amdgcn_raw_buffer_load_b128(rs, voff + 3072, 0, KV_AUX);
  return p;
}

struct QFrag { v4u q[2]; };
__device__ __forceinline__ f32x4 qk_page(const Page& k, const QFrag& q) {
  const f32x4 z = {0.f, 0.f, 0.f, 0.f};
  f32x4 d0 = mfma(k.r[0], q.q[0], z);
  f32x4 d1 = mfma(k.r[2], q.q[0], z);
  d0 = mfma(k.r[1], q.q[1], d0);
  d1 = mfma(k.r[3], q.q[1], d1);
  f32x4 x;
#pragma unroll
  for (int r = 0; r < 4; ++r) {
    auto s = __builtin_amdgcn_permlane32_swap(__float_as_uint(d0[r]), __float_as_uint(d1[r]), false, false);
    x[r] = __uint_as_float(s[0]) + dpp<0x128>(__uint_as_float(s[1]));
  }
  return x;
}

struct State { float m, l; f32x4 acc[8]; };

template <bool FIRST>
__device__ __forceinline__ v4u softmax_step(State& st, f32x4 xa, f32x4 xb, int lima, int limb) {
#pragma unroll
  for (int r = 0; r < 4; ++r) {
    xa[r] = (r < lima) ? xa[r] : -INFINITY;
    xb[r] = (r < limb) ? xb[r] : -INFINITY;
  }
  float mx = fmaxf(fmaxf(fmaxf(xa[0], xa[1]), fmaxf(xa[2], xa[3])), fmaxf(fmaxf(xb[0], xb[1]), fmaxf(xb[2], xb[3])));
  mx = xg_max(mx);
  float mn = FIRST ? mx : fmaxf(st.m, mx);
  const float mc = mn * C2;
  float pa[4], pb[4];
#pragma unroll
  for (int r = 0; r < 4; ++r) {
    pa[r] = __builtin_amdgcn_exp2f(fmaf(xa[r], C2, -mc));
    pb[r] = __builtin_amdgcn_exp2f(fmaf(xb[r], C2, -mc));
  }
  const float ps = ((pa[0] + pa[1]) + (pa[2] + pa[3])) + ((pb[0] + pb[1]) + (pb[2] + pb[3]));
  if (FIRST) {
    st.l = ps;
  } else {
    const float alpha = __builtin_amdgcn_exp2f(fmaf(st.m, C2, -mc));
    st.l = fmaf(st.l, alpha, ps);
#pragma unroll
    for (int m = 0; m < 8; ++m) st.acc[m] *= alpha;
  }
  st.m = mn;
  v4u p;
  p[0] = cvtpk(pa[0], pa[1]);
  p[1] = cvtpk(pa[2], pa[3]);
  p[2] = cvtpk(pb[0], pb[1]);
  p[3] = cvtpk(pb[2], pb[3]);
  return p;
}

template <bool FIRST>
__device__ __forceinline__ void pv_step(State& st, const Page& va, const Page& vb, v4u p) {
#pragma unroll
  for (int m = 0; m < 8; ++m) {
    const int w = m >> 1;
    const unsigned sel = (m & 1) ? 0x07060302u : 0x05040100u;
    v4u f;
    f[0] = __builtin_amdgcn_perm(va.r[1][w], va.r[0][w], sel);
    f[1] = __builtin_amdgcn_perm(va.r[3][w], va.r[2][w], sel);
    f[2] = __builtin_amdgcn_perm(vb.r[1][w], vb.r[0][w], sel);
    f[3] = __builtin_amdgcn_perm(vb.r[3][w], vb.r[2][w], sel);
    if (FIRST) {
      const f32x4 z = {0.f, 0.f, 0.f, 0.f};
      st.acc[m] = mfma(f, p, z);
    } else {
      st.acc[m] = mfma(f, p, st.acc[m]);
    }
  }
}

constexpr int LDS_ROW = D + 4;

#define SB() do { asm volatile("" ::: "memory"); __builtin_amdgcn_sched_barrier(0); } while (0)

__global__ void __launch_bounds__(NTHR, 2)
attn_split(const __hip_bfloat16* q, const char* kc, const char* vc,
           const int* __restrict__ bt, const int* __restrict__ ctx_lens, float* __restrict__ po, float* __restrict__ pml) {
  __shared__ __attribute__((aligned(16))) float lds[NWAVE * G * LDS_ROW];
  TS(0);
  const int tid = threadIdx.x, lane = tid & 63;
  const int w = __builtin_amdgcn_readfirstlane(tid >> 6);
  const int kvh = (blockIdx.x >> 3) & 3, s = (blockIdx.x & 7) + 8 * (blockIdx.x >> 5);
  const int n = lane & 15, g = lane >> 4;

  int phys[UPW];
#pragma unroll
  for (int i = 0; i < UPW; ++i) phys[i] = bt[(i * NWAVE + w) * NSPLIT + s];
  int ctx = ctx_lens[0];
  ctx = ctx < 0 ? 0 : (ctx > LK ? LK : ctx);

  QFrag qf;
  const char* qrow = (const char*)q + (size_t)(kvh * G + (n & 7)) * (D * 2);
  qf.q[0] = *(const v4u*)(qrow + (n >> 3) * 64 + g * 16);
  qf.q[1] = *(const v4u*)(qrow + 128 + (n >> 3) * 64 + g * 16);
  const int koff = (n & 7) * 1024 + (n >> 3) * 64 + g * 16;
  const int voff = g * 4096 + n * 16;
  const char* kb = kc + kvh * 256;
  const char* vb = vc + kvh * 256;

  int rem[UPW];
#pragma unroll
  for (int i = 0; i < UPW; ++i) rem[i] = ctx - ((i * NWAVE + w) * NSPLIT + s) * BS;
  const int g4 = 4 * g;

  State st;
  st.m = -INFINITY; st.l = 0.f;
#pragma unroll
  for (int m = 0; m < 8; ++m) st.acc[m] = f32x4{0.f, 0.f, 0.f, 0.f};

  asm volatile("" :: "s"(phys[0]), "s"(phys[1]), "s"(phys[2]), "s"(phys[3]), "s"(phys[4]), "s"(phys[5]), "s"(phys[6]), "s"(phys[7]), "s"(ctx));
  SB();
  TS(1);
  Page K0 = load_k(kb, phys[0], rem[0] > 0, koff);
  Page K1 = load_k(kb, phys[1], rem[1] > 0, koff);
  Page V0 = load_v(vb, phys[0], rem[0] > 0, voff);
  Page V1 = load_v(vb, phys[1], rem[1] > 0, voff);
  Page K2 = load_k(kb, phys[2], rem[2] > 0, koff);
  Page K3 = load_k(kb, phys[3], rem[3] > 0, koff);
  Page V2 = load_v(vb, phys[2], rem[2] > 0, voff);
  Page V3 = load_v(vb, phys[3], rem[3] > 0, voff);
  Page K4 = load_k(kb, phys[4], rem[4] > 0, koff);
  Page K5 = load_k(kb, phys[5], rem[5] > 0, koff);
  SB();
  TS(2);
  if (rem[0] > 0) {
    f32x4 xa = qk_page(K0, qf), xb = qk_page(K1, qf);
    SB();
    TS(3);
    Page V4 = load_v(vb, phys[4], rem[4] > 0, voff);
    Page V5 = load_v(vb, phys[5], rem[5] > 0, voff);
    SB();
    v4u p = softmax_step<true>(st, xa, xb, rem[0] - g4, rem[1] - g4);
    pv_step<true>(st, V0, V1, p);
    SB();
    TS(4);
    Page K6 = load_k(kb, phys[6], rem[6] > 0, koff);
    Page K7 = load_k(kb, phys[7], rem[7] > 0, koff);
    SB();
    if (rem[2] > 0) {
      xa = qk_page(K2, qf); xb = qk_page(K3, qf);
      SB();
      Page V6 = load_v(vb, phys[6], rem[6] > 0, voff);
      Page V7 = load_v(vb, phys[7], rem[7] > 0, voff);
      SB();
      p = softmax_step<false>(st, xa, xb, rem[2] - g4, rem[3] - g4);
      pv_step<false>(st, V2, V3, p);
      SB();
      TS(5);
      if (rem[4] > 0) {
        xa = qk_page(K4, qf); xb = qk_page(K5, qf);
        p = softmax_step<false>(st, xa, xb, rem[4] - g4, rem[5] - g4);
        pv_step<false>(st, V4, V5, p);
        SB();
        TS(6);
        if (rem[6] > 0) {
          xa = qk_page(K6, qf); xb = qk_page(K7, qf);
          p = softmax_step<false>(st, xa, xb, rem[6] - g4, rem[7] - g4);
          pv_step<false>(st, V6, V7, p);
          TS(7);
        }
      }
    }
  }
  const float lsum = xg_sum(st.l);
  if (n < G) {
    float* rec = lds + (w * G + n) * LDS_ROW;
#pragma unroll
    for (int r = 0; r < 4; ++r) {
      *(f32x4*)(rec + 32 * g + 8 * r) = f32x4{st.acc[0][r], st.acc[1][r], st.acc[2][r], st.acc[3][r]};
      *(f32x4*)(rec + 32 * g + 8 * r + 4) = f32x4{st.acc[4][r], st.acc[5][r], st.acc[6][r], st.acc[7][r]};
    }
    if (g == 0) { rec[D] = st.m; rec[D + 1] = lsum; }
  }
  TS(8);
  __syncthreads();
  TS(9);
  {
    const int hh = w;
    float mw[NWAVE], lw[NWAVE];
    float M = -INFINITY;
#pragma unroll
    for (int i = 0; i < NWAVE; ++i) {
      const f32x2 ml = *(const f32x2*)(lds + (i * G + hh) * LDS_ROW + D);
      mw[i] = ml[0]; lw[i] = ml[1];
      M = fmaxf(M, mw[i]);
    }
    const float Mc = (M == -INFINITY) ? 0.f : M * C2;
    float L = 0.f, o0 = 0.f, o1 = 0.f;
#pragma unroll
    for (int i = 0; i < NWAVE; ++i) {
      const float e = __builtin_amdgcn_exp2f(fmaf(mw[i], C2, -Mc));
      const f32x2 a = *(const f32x2*)(lds + (i * G + hh) * LDS_ROW + 2 * lane);
      L = fmaf(e, lw[i], L); o0 = fmaf(e, a[0], o0); o1 = fmaf(e, a[1], o1);
    }
    const int slot = (kvh * G + hh) * NSPLIT + s;
    *(f32x2*)(po + (size_t)slot * D + 2 * lane) = f32x2{o0, o1};
    if (lane == 0) *(f32x2*)(pml + 2 * slot) = f32x2{M, L};
  }
}

__global__ void __launch_bounds__(256)
attn_combine(const float* po, const float* __restrict__ pml, const __hip_bfloat16* __restrict__ vcache,
             const int* __restrict__ bt, const int* __restrict__ ctx_lens, __hip_bfloat16* __restrict__ out) {
  __shared__ __attribute__((aligned(16))) float red[32 * 36];
  TS(0);
  const int tid = threadIdx.x, lane = tid & 63;
  const int h = blockIdx.x >> 2, ds = blockIdx.x & 3;
  const int c = tid & 7, sg = tid >> 3;
  const f32x4 a0 = *(const f32x4*)(po + (size_t)(h * NSPLIT + sg) * D + ds * 32 + c * 4);
  const f32x4 a1 = *(const f32x4*)(po + (size_t)(h * NSPLIT + sg + 32) * D + ds * 32 + c * 4);
  const f32x2 ml = *(const f32x2*)(pml + 2 * (h * NSPLIT + lane));
  int ctx = ctx_lens[0];
  ctx = ctx < 0 ? 0 : (ctx > LK ? LK : ctx);
  float M = ml[0];
#pragma unroll
  for (int o = 1; o < 64; o <<= 1) M = fmaxf(M, __shfl_xor(M, o));
  const float Mc = (M == -INFINITY) ? 0.f : M * C2;
  const float e = __builtin_amdgcn_exp2f(fmaf(ml[0], C2, -Mc));
  float L = e * ml[1];
#pragma unroll
  for (int o = 1; o < 64; o <<= 1) L += __shfl_xor(L, o);
  const float e0 = __shfl(e, sg), e1 = __shfl(e, sg + 32);
  f32x4 o4 = a0 * e0 + a1 * e1;
  const float Mnat = M * SCALE;
  float wv = 1.f, wm = 0.f;
  const bool slow = (ctx < LK) && !(Mnat > -1.0e9f + 512.f);
  if (slow) {
    const float Mall = fmaxf(Mnat, -1.0e9f);
    wv = (M == -INFINITY) ? 0.f : __expf(Mnat - Mall);
    wm = __expf(-1.0e9f - Mall);
    f32x4 sv = {0.f, 0.f, 0.f, 0.f};
    const int kvh = h / G;
    for (int t = ctx + sg; t < LK; t += 32) {
      const __hip_bfloat16* vp = vcache + ((size_t)bt[t / BS] * BS + (t % BS)) * (HKV * D) + kvh * D + ds * 32 + c * 4;
#pragma unroll
      for (int j = 0; j < 4; ++j) sv[j] += __bfloat162float(vp[j]);
    }
    o4 = o4 * wv + sv * wm;
    L = L * wv + wm * (float)(LK - ctx);
  }
  TS(1);
  *(f32x4*)(red + sg * 36 + c * 4) = o4;
  __syncthreads();
  if (tid < 32) {
    float acc = 0.f;
#pragma unroll
    for (int i = 0; i < 32; ++i) acc += red[i * 36 + tid];
    out[h * D + ds * 32 + tid] = __float2bfloat16(acc / L);
  }
}

}

extern "C" void kernel_launch(void* const* d_in, const int* in_sizes, int n_in, void* d_out, int out_size, void* d_ws,
                              size_t ws_size, hipStream_t stream) {
  const __hip_bfloat16* q = (const __hip_bfloat16*)d_in[0];
  const char* kc = (const char*)d_in[1];
  const char* vc = (const char*)d_in[2];
  const int* bt = (const int*)d_in[3];
  const int* ctx = (const int*)d_in[4];
  float* po = (float*)d_ws;
  float* pml = po + (size_t)H * NSPLIT * D;
  attn_split<<<HKV * NSPLIT, NTHR, 0, stream>>>(q, kc, vc, bt, ctx, po, pml);
  attn_combine<<<H * 4, 256, 0, stream>>>(po, pml, (const __hip_bfloat16*)vc, bt, ctx, (__hip_bfloat16*)d_out);
}
```

```cpp
#include <hip/hip_runtime.h>
#include <hip/hip_bf16.h>
#include <stdint.h>

#define POLL_START 4u
#define FEW 12
#define POLL_SLEEP 0
#define KV_AUX 2

#define TS(i)
#define FLUSH_STAMPS()

namespace {

constexpr int H = 32, HKV = 4, D = 128, BS = 16, NB = 4096, G = H / HKV;
constexpr int LK = NB * BS;
constexpr int NWG = 256, NWAVE = 8, NTHR = NWAVE * 64;
constexpr int UPW = NB / (NWG * 2);
static_assert(UPW == 8, "straight-line body is written for 8 pages per wave");
constexpr float SCALE = 0.08838834764831845f;
constexpr float LOG2E = 1.4426950408889634f;
constexpr float C2 = SCALE * LOG2E;
constexpr int OOB = 0x40000000;
constexpr int WS_VAL = 4096;
constexpr int WS_BYTES = WS_VAL + H * 4 * NWG * 128;
constexpr int AUX_SC1 = 16;

typedef unsigned v4u __attribute__((ext_vector_type(4)));
typedef unsigned v2u __attribute__((ext_vector_type(2)));
typedef float f32x4 __attribute__((ext_vector_type(4)));
typedef __bf16 bf16x8 __attribute__((ext_vector_type(8)));
typedef __bf16 bf16x2 __attribute__((ext_vector_type(2)));
typedef float f32x2 __attribute__((ext_vector_type(2)));
#define LAS __attribute__((address_space(3)))
#define CAS __attribute__((address_space(4)))

__device__ __forceinline__ f32x4 mfma(v4u a, v4u b, f32x4 c) {
  return __builtin_amdgcn_mfma_f32_16x16x32_bf16(__builtin_bit_cast(bf16x8, a), __builtin_bit_cast(bf16x8, b), c, 0, 0, 0);
}
__device__ __forceinline__ unsigned cvtpk(float lo, float hi) {
  f32x2 v = {lo, hi};
  bf16x2 r = __builtin_convertvector(v, bf16x2);
  return __builtin_bit_cast(unsigned, r);
}
template <int CTRL> __device__ __forceinline__ float dpp(float x) {
  return __builtin_bit_cast(float, __builtin_amdgcn_mov_dpp(__builtin_bit_cast(int, x), CTRL, 0xf, 0xf, true));
}
__device__ __forceinline__ float xg_max(float x) {
  auto s = __builtin_amdgcn_permlane16_swap(__float_as_uint(x), __float_as_uint(x), false, false);
  x = fmaxf(__uint_as_float(s[0]), __uint_as_float(s[1]));
  auto t = __builtin_amdgcn_permlane32_swap(__float_as_uint(x), __float_as_uint(x), false, false);
  return fmaxf(__uint_as_float(t[0]), __uint_as_float(t[1]));
}
__device__ __forceinline__ float xg_sum(float x) {
  auto s = __builtin_amdgcn_permlane16_swap(__float_as_uint(x), __float_as_uint(x), false, false);
  x = __uint_as_float(s[0]) + __uint_as_float(s[1]);
  auto t = __builtin_amdgcn_permlane32_swap(__float_as_uint(x), __float_as_uint(x), false, false);
  return __uint_as_float(t[0]) + __uint_as_float(t[1]);
}
__device__ __forceinline__ float wave_max(float x) {
  x = fmaxf(x, dpp<0xB1>(x)); x = fmaxf(x, dpp<0x4E>(x)); x = fmaxf(x, dpp<0x141>(x)); x = fmaxf(x, dpp<0x140>(x));
  return xg_max(x);
}
__device__ __forceinline__ float wave_sum(float x) {
  x += dpp<0xB1>(x); x += dpp<0x4E>(x); x += dpp<0x141>(x); x += dpp<0x140>(x);
  return xg_sum(x);
}

__device__ __forceinline__ float quad_col_sum(float x) {
  x += dpp<0x124>(x); x += dpp<0x128>(x);
  return xg_sum(x);
}

struct Page { v4u r[4]; };

__device__ __forceinline__ Page load_k(__amdgpu_buffer_rsrc_t rs, int phys, bool valid, int voff) {
  const int so = phys << 14;
  const int vo = voff | (valid ? 0 : OOB);
  Page p;
  p.r[0] = __builtin_amdgcn_raw_buffer_load_b128(rs, vo, so, KV_AUX);
  p.r[1] = __builtin_amdgcn_raw_buffer_load_b128(rs, vo + 128, so, KV_AUX);
  p.r[2] = __builtin_amdgcn_raw_buffer_load_b128(rs, vo, so + 8192, KV_AUX);
  p.r[3] = __builtin_amdgcn_raw_buffer_load_b128(rs, vo + 128, so + 8192, KV_AUX);
  return p;
}
__device__ __forceinline__ Page load_v(__amdgpu_buffer_rsrc_t rs, int phys, bool valid, int voff) {
  const int so = phys << 14;
  const int vo = voff | (valid ? 0 : OOB);
  Page p;
  p.r[0] = __builtin_amdgcn_raw_buffer_load_b128(rs, vo, so, KV_AUX);
  p.r[1] = __builtin_amdgcn_raw_buffer_load_b128(rs, vo + 1024, so, KV_AUX);
  p.r[2] = __builtin_amdgcn_raw_buffer_load_b128(rs, vo + 2048, so, KV_AUX);
  p.r[3] = __builtin_amdgcn_raw_buffer_load_b128(rs, vo + 3072, so, KV_AUX);
  return p;
}

struct QFrag { v4u q[2]; };
__device__ __forceinline__ f32x4 qk_page(const Page& k, const QFrag& q) {
  const f32x4 z = {0.f, 0.f, 0.f, 0.f};
  f32x4 d0 = mfma(k.r[0], q.q[0], z);
  f32x4 d1 = mfma(k.r[2], q.q[0], z);
  d0 = mfma(k.r[1], q.q[1], d0);
  d1 = mfma(k.r[3], q.q[1], d1);
  f32x4 x;
#pragma unroll
  for (int r = 0; r < 4; ++r) {
    auto s = __builtin_amdgcn_permlane32_swap(__float_as_uint(d0[r]), __float_as_uint(d1[r]), false, false);
    x[r] = __uint_as_float(s[0]) + dpp<0x128>(__uint_as_float(s[1]));
  }
  return x;
}

struct State { float m, l; f32x4 acc[8]; };

template <bool FIRST>
__device__ __forceinline__ v4u softmax_step(State& st, f32x4 xa, f32x4 xb, int lima, int limb) {
#pragma unroll
  for (int r = 0; r < 4; ++r) {
    xa[r] = (r < lima) ? xa[r] : -INFINITY;
    xb[r] = (r < limb) ? xb[r] : -INFINITY;
  }
  float mx = fmaxf(fmaxf(fmaxf(xa[0], xa[1]), fmaxf(xa[2], xa[3])), fmaxf(fmaxf(xb[0], xb[1]), fmaxf(xb[2], xb[3])));
  mx = xg_max(mx);
  float mn = FIRST ? mx : fmaxf(st.m, mx);
  const float mc = mn * C2;
  float pa[4], pb[4];
#pragma unroll
  for (int r = 0; r < 4; ++r) {
    pa[r] = __builtin_amdgcn_exp2f(fmaf(xa[r], C2, -mc));
    pb[r] = __builtin_amdgcn_exp2f(fmaf(xb[r], C2, -mc));
  }
  const float ps = ((pa[0] + pa[1]) + (pa[2] + pa[3])) + ((pb[0] + pb[1]) + (pb[2] + pb[3]));
  if (FIRST) {
    st.l = ps;
  } else {
    const float alpha = __builtin_amdgcn_exp2f(fmaf(st.m, C2, -mc));
    st.l = fmaf(st.l, alpha, ps);
#pragma unroll
    for (int m = 0; m < 8; ++m) st.acc[m] *= alpha;
  }
  st.m = mn;
  v4u p;
  p[0] = cvtpk(pa[0], pa[1]);
  p[1] = cvtpk(pa[2], pa[3]);
  p[2] = cvtpk(pb[0], pb[1]);
  p[3] = cvtpk(pb[2], pb[3]);
  return p;
}

template <bool FIRST>
__device__ __forceinline__ void pv_step(State& st, const Page& va, const Page& vb, v4u p) {
#pragma unroll
  for (int m = 0; m < 8; ++m) {
    const int w = m >> 1;
    const unsigned sel = (m & 1) ? 0x07060302u : 0x05040100u;
    v4u f;
    f[0] = __builtin_amdgcn_perm(va.r[1][w], va.r[0][w], sel);
    f[1] = __builtin_amdgcn_perm(va.r[3][w], va.r[2][w], sel);
    f[2] = __builtin_amdgcn_perm(vb.r[1][w], vb.r[0][w], sel);
    f[3] = __builtin_amdgcn_perm(vb.r[3][w], vb.r[2][w], sel);
    if (FIRST) {
      const f32x4 z = {0.f, 0.f, 0.f, 0.f};
      st.acc[m] = mfma(f, p, z);
    } else {
      st.acc[m] = mfma(f, p, st.acc[m]);
    }
  }
}

constexpr int REC_ROW = D + 4;
constexpr int REC = G * REC_ROW;
constexpr int T_ROW = 68;
constexpr int LDS_CTL = HKV * REC;
constexpr int LDS_FIN = LDS_CTL + 8;
constexpr int FIN_ROW = 36;
constexpr int LDS_WORDS = LDS_FIN + 5 * FIN_ROW;

__device__ __forceinline__ void merge_record(State& st, const float* rec, int g) {
  f32x4 o[8];
#pragma unroll
  for (int r = 0; r < 4; ++r) {
    o[2 * r] = *(const f32x4*)(rec + 32 * g + 8 * r);
    o[2 * r + 1] = *(const f32x4*)(rec + 32 * g + 8 * r + 4);
  }
  const f32x2 ml = *(const f32x2*)(rec + D);
  const float M = fmaxf(st.m, ml[0]);
  const float Mc = (M == -INFINITY) ? 0.f : M * C2;
  const float e1 = __builtin_amdgcn_exp2f(fmaf(st.m, C2, -Mc)), e2 = __builtin_amdgcn_exp2f(fmaf(ml[0], C2, -Mc));
  st.l = fmaf(st.l, e1, g == 0 ? e2 * ml[1] : 0.f);
  st.m = M;
#pragma unroll
  for (int r = 0; r < 4; ++r)
#pragma unroll
    for (int m = 0; m < 8; ++m) st.acc[m][r] = fmaf(st.acc[m][r], e1, o[2 * r + (m >> 2)][m & 3] * e2);
}

#define SB() do { asm volatile("" ::: "memory"); __builtin_amdgcn_sched_barrier(0); } while (0)
#define LGKM0() asm volatile("s_waitcnt lgkmcnt(0)" ::: "memory")

__global__ void __launch_bounds__(NTHR, 2)
attn_decode(const char* q, const char* kc, const char* vc, const int* __restrict__ bt, const int* __restrict__ ctx_lens,
            char* ws, char* out, int kv_bytes, int pad_) {
  __shared__ __attribute__((aligned(16))) float lds[LDS_WORDS];
  const int tid = threadIdx.x, lane = tid & 63;
  const int w = __builtin_amdgcn_readfirstlane(tid >> 6);
  TS(0);
  const int kvh = w & 3, sub = w >> 2, c = blockIdx.x;
  const int n = lane & 15, g = lane >> 4;
  asm volatile("" :: "s"(q), "s"(kc), "s"(vc), "s"(bt), "s"(ctx_lens), "s"(ws), "s"(out), "s"(kv_bytes));

  int phys[UPW];
  const CAS int* btp = (const CAS int*)bt + (unsigned)(sub * NWG + c);
#pragma unroll
  for (int i = 0; i < UPW; ++i) phys[i] = btp[i * 2 * NWG];
  int ctx = *(const CAS int*)ctx_lens;
  const unsigned ep = *(const CAS unsigned*)ws;
  volatile LAS unsigned* ctl = (volatile LAS unsigned*)(lds + LDS_CTL);
  if (tid < 8) ctl[tid] = 0u;

  QFrag qf;
  const char* qrow = q + (kvh * G + (n & 7)) * (D * 2) + (n >> 3) * 64 + g * 16;
  qf.q[0] = *(const v4u*)(qrow);
  qf.q[1] = *(const v4u*)(qrow + 128);
  const int koff = (n & 7) * 1024 + (n >> 3) * 64 + g * 16;
  const int voff = g * 4096 + n * 16;
  const __amdgpu_buffer_rsrc_t rk = __builtin_amdgcn_make_buffer_rsrc((void*)(kc + kvh * 256), 0, kv_bytes - kvh * 256, 0x00020000);
  const __amdgpu_buffer_rsrc_t rv = __builtin_amdgcn_make_buffer_rsrc((void*)(vc + kvh * 256), 0, kv_bytes - kvh * 256, 0x00020000);

  asm volatile("" :: "s"(phys[0]), "s"(phys[1]), "s"(phys[2]), "s"(phys[3]), "s"(phys[4]), "s"(phys[5]), "s"(phys[6]), "s"(phys[7]),
               "s"(ctx), "s"(ep));
  LGKM0();
  __builtin_amdgcn_s_barrier();
  SB();
  ctx = ctx < 0 ? 0 : (ctx > LK ? LK : ctx);
  int rem[UPW];
#pragma unroll
  for (int i = 0; i < UPW; ++i) rem[i] = ctx - ((i * 2 + sub) * NWG + c) * BS;
  const int g4 = 4 * g;

  State st;
  st.m = -INFINITY; st.l = 0.f;
#pragma unroll
  for (int m = 0; m < 8; ++m) st.acc[m] = f32x4{0.f, 0.f, 0.f, 0.f};

  float* rec = lds + kvh * REC + (n & 7) * REC_ROW;
  int merged = 0;
  TS(1);
  Page K0 = load_k(rk, phys[0], rem[0] > 0, koff);
  Page K1 = load_k(rk, phys[1], rem[1] > 0, koff);
  Page V0 = load_v(rv, phys[0], rem[0] > 0, voff);
  Page V1 = load_v(rv, phys[1], rem[1] > 0, voff);
  Page K2 = load_k(rk, phys[2], rem[2] > 0, koff);
  Page K3 = load_k(rk, phys[3], rem[3] > 0, koff);
  Page V2 = load_v(rv, phys[2], rem[2] > 0, voff);
  Page V3 = load_v(rv, phys[3], rem[3] > 0, voff);
  Page K4 = load_k(rk, phys[4], rem[4] > 0, koff);
  Page K5 = load_k(rk, phys[5], rem[5] > 0, koff);
  SB();
  TS(2);
  if (rem[0] > 0) {
    f32x4 xa = qk_page(K0, qf), xb = qk_page(K1, qf);
    SB();
    Page V4 = load_v(rv, phys[4], rem[4] > 0, voff);
    Page V5 = load_v(rv, phys[5], rem[5] > 0, voff);
    SB();
    v4u p = softmax_step<true>(st, xa, xb, rem[0] - g4, rem[1] - g4);
    pv_step<true>(st, V0, V1, p);
    SB();
    TS(3);
    Page K6 = load_k(rk, phys[6], rem[6] > 0, koff);
    Page K7 = load_k(rk, phys[7], rem[7] > 0, koff);
    SB();
    if (rem[2] > 0) {
      xa = qk_page(K2, qf); xb = qk_page(K3, qf);
      SB();
      Page V6 = load_v(rv, phys[6], rem[6] > 0, voff);
      Page V7 = load_v(rv, phys[7], rem[7] > 0, voff);
      SB();
      p = softmax_step<false>(st, xa, xb, rem[2] - g4, rem[3] - g4);
      pv_step<false>(st, V2, V3, p);
      SB();
      TS(4);
      if (rem[4] > 0) {
        if (__builtin_amdgcn_readfirstlane(ctl[kvh]) == 3u) {
          asm volatile("" ::: "memory");
          merge_record(st, rec, g);
          merged = 1;
        }
        SB();
        xa = qk_page(K4, qf); xb = qk_page(K5, qf);
        p = softmax_step<false>(st, xa, xb, rem[4] - g4, rem[5] - g4);
        pv_step<false>(st, V4, V5, p);
        SB();
        TS(5);
        if (rem[6] > 0) {
          xa = qk_page(K6, qf); xb = qk_page(K7, qf);
          p = softmax_step<false>(st, xa, xb, rem[6] - g4, rem[7] - g4);
          pv_step<false>(st, V6, V7, p);
        }
      }
    }
  }
  TS(6);
  const unsigned e1_ = ep + 1u, hA = e1_ * 0x9E3779B1u, KX = (e1_ << 1) | 1u, hB = hA ^ KX;
  const __amdgpu_buffer_rsrc_t rw = __builtin_amdgcn_make_buffer_rsrc((void*)ws, 0, WS_BYTES, 0x00020000);
  const __amdgpu_buffer_rsrc_t ro = __builtin_amdgcn_make_buffer_rsrc((void*)out, 0, H * D * 2, 0x00020000);
  unsigned t = 1;
  if (!merged) {
    t = 0;
    if (lane == 0) t = __hip_atomic_fetch_add((LAS unsigned*)(ctl + kvh), 1u, __ATOMIC_RELAXED, __HIP_MEMORY_SCOPE_WORKGROUP);
    t = __builtin_amdgcn_readfirstlane(t);
    if (t != 0) {
      while (t != 3u && t != 4u) { __builtin_amdgcn_s_sleep(1); t = __builtin_amdgcn_readfirstlane(ctl[kvh]); }
      asm volatile("" ::: "memory");
      merge_record(st, rec, g);
    }
  }
  const float lsum = xg_sum(st.l);
  if (t != 0) {
    const float inv = (lsum > 0.f) ? __builtin_amdgcn_rcpf(lsum) : 0.f;
    const float lse2 = (lsum > 0.f) ? fmaf(st.m, C2, __builtin_amdgcn_logf(lsum)) : -INFINITY;
    unsigned* tr = (unsigned*)(lds + kvh * REC);
    if (n < G) {
#pragma unroll
      for (int r = 0; r < 4; ++r) {
        v4u wv;
#pragma unroll
        for (int x = 0; x < 4; ++x) wv[x] = cvtpk(st.acc[2 * x][r] * inv, st.acc[2 * x + 1][r] * inv);
        *(v4u*)(tr + n * T_ROW + 16 * g + 4 * r) = wv;
      }
      if (g == 0) tr[n * T_ROW + 64] = __float_as_uint(lse2);
    }
    const int gi = lane & 31;
    const int vo = WS_VAL + ((((kvh * G + (lane >> 5)) * 4 + (gi >> 3)) * NWG + c) * 8 + (gi & 7)) * 16;
#pragma unroll
    for (int k = 0; k < 4; ++k) {
      const v2u d = *(const v2u*)(tr + (2 * k + (lane >> 5)) * T_ROW + 2 * gi);
      const unsigned lb = tr[(2 * k + (lane >> 5)) * T_ROW + 64];
      const v4u gr = {d[0], lb ^ hA, d[1], lb ^ hB};
      __builtin_amdgcn_raw_buffer_store_b128(gr, rw, vo + k * (2 * 4 * NWG * 128), 0, AUX_SC1);
    }
    if (lane == 0) __hip_atomic_fetch_add((LAS unsigned*)(ctl + 4), 1u, __ATOMIC_RELAXED, __HIP_MEMORY_SCOPE_WORKGROUP);
    TS(7);
  } else {
    if (n < G) {
#pragma unroll
      for (int r = 0; r < 4; ++r) {
        *(f32x4*)(rec + 32 * g + 8 * r) = f32x4{st.acc[0][r], st.acc[1][r], st.acc[2][r], st.acc[3][r]};
        *(f32x4*)(rec + 32 * g + 8 * r + 4) = f32x4{st.acc[4][r], st.acc[5][r], st.acc[6][r], st.acc[7][r]};
      }
      if (g == 0) *(f32x2*)(rec + D) = f32x2{st.m, lsum};
    }
    LGKM0();
    if (lane == 0) __hip_atomic_fetch_add((LAS unsigned*)(ctl + kvh), 2u, __ATOMIC_RELAXED, __HIP_MEMORY_SCOPE_WORKGROUP);
    TS(8);
    if (c >= 128) { FLUSH_STAMPS(); return; }
    const int h = c >> 2, u = c & 3;
    const int vo = WS_VAL + (c * NWG + 64 * kvh) * 128 + lane * 16;
    float gl[8]; unsigned g0[8], g1[8];
    bool ok[8];
#pragma unroll
    for (int i = 0; i < 8; ++i) { gl[i] = 0.f; g0[i] = 0u; g1[i] = 0u; ok[i] = false; }
    int first = 1;
    unsigned pass = 0;
#pragma clang loop unroll(disable)
    for (;;) {
      v4u nv[8];
      int ov[8];
#pragma unroll
      for (int i = 0; i < 8; ++i) ov[i] = (ok[i] ? OOB : vo) + i * 1024;
      SB();
#pragma unroll
      for (int i = 0; i < 8; ++i) nv[i] = __builtin_amdgcn_raw_buffer_load_b128(rw, ov[i], 0, AUX_SC1);
      SB();
      bool all = true;
#pragma unroll
      for (int i = 0; i < 8; ++i) {
        const unsigned x = (nv[i][1] ^ nv[i][3] ^ KX) | (nv[i][1] ^ (unsigned)__builtin_amdgcn_mov_dpp((int)nv[i][1], 0xB1, 0xf, 0xf, true));
        const unsigned y = x | (unsigned)__builtin_amdgcn_mov_dpp((int)x, 0xB1, 0xf, 0xf, true);
        const bool nw = (y == 0u);
        gl[i] = nw ? __uint_as_float(nv[i][1] ^ hA) : gl[i];
        g0[i] = nw ? nv[i][0] : g0[i];
        g1[i] = nw ? nv[i][2] : g1[i];
        ok[i] = ok[i] || nw;
        all = all && ok[i];
      }
      const bool done = __builtin_amdgcn_ballot_w64(!all) == 0ull;
      { int fv = first; asm volatile("" : "+v"(fv)); first = __builtin_amdgcn_readfirstlane(fv); }
      if (done || first) {
        float M = fmaxf(fmaxf(fmaxf(gl[0], gl[1]), fmaxf(gl[2], gl[3])), fmaxf(fmaxf(gl[4], gl[5]), fmaxf(gl[6], gl[7])));
        M = xg_max(fmaxf(M, dpp<0x128>(M)));
        const float Ms = (M == -INFINITY) ? 0.f : M;
        float W = 0.f, a0 = 0.f, a1 = 0.f, a2 = 0.f, a3 = 0.f;
#pragma unroll
        for (int i = 0; i < 8; ++i) {
          const float e = __builtin_amdgcn_exp2f(gl[i] - Ms);
          W += e;
          a0 = fmaf(e, __uint_as_float(g0[i] << 16), a0); a1 = fmaf(e, __uint_as_float(g0[i] & 0xffff0000u), a1);
          a2 = fmaf(e, __uint_as_float(g1[i] << 16), a2); a3 = fmaf(e, __uint_as_float(g1[i] & 0xffff0000u), a3);
        }
        W = xg_sum(W + dpp<0x128>(W));
        a0 = xg_sum(a0 + dpp<0x128>(a0)); a1 = xg_sum(a1 + dpp<0x128>(a1));
        a2 = xg_sum(a2 + dpp<0x128>(a2)); a3 = xg_sum(a3 + dpp<0x128>(a3));
        float* slot = lds + LDS_FIN + (done ? kvh : 4) * FIN_ROW;
        if (lane < 8) {
          *(f32x4*)(slot + 4 * lane) = f32x4{a0, a1, a2, a3};
          if (lane == 0) *(f32x2*)(slot + 32) = f32x2{M, W};
        }
        LGKM0();
        unsigned tk = 0;
        if (lane == 0) tk = __hip_atomic_fetch_add((LAS unsigned*)(ctl + (done ? 5 : 6)), 1u, __ATOMIC_RELAXED, __HIP_MEMORY_SCOPE_WORKGROUP);
        tk = __builtin_amdgcn_readfirstlane(tk);
        if (done && tk != 3u) {
          TS(11);
          FLUSH_STAMPS();
          return;
        }
        asm volatile("" ::: "memory");
        const float* fin = lds + LDS_FIN;
        const int dd = lane & 31;
        float mw[4], ww[4], av[4];
#pragma unroll
        for (int i = 0; i < 4; ++i) {
          const f32x2 x = *(const f32x2*)(fin + i * FIN_ROW + 32);
          mw[i] = x[0]; ww[i] = x[1]; av[i] = fin[i * FIN_ROW + dd];
        }
        M = fmaxf(fmaxf(mw[0], mw[1]), fmaxf(mw[2], mw[3]));
        const float Mf = (M == -INFINITY) ? 0.f : M;
        W = 0.f; a0 = 0.f;
#pragma unroll
        for (int i = 0; i < 4; ++i) {
          const float f = __builtin_amdgcn_exp2f(mw[i] - Mf);
          W = fmaf(f, ww[i], W); a0 = fmaf(f, av[i], a0);
        }
        if (done) {
          if (ctx < LK && !(M > -1.0e9f * LOG2E + 2048.f)) {
            const float lm = -1.0e9f * LOG2E;
            const float Mall = fmaxf(M, lm);
            const float wv = (M == -INFINITY) ? 0.f : __builtin_amdgcn_exp2f(M - Mall);
            const float wm = __builtin_amdgcn_exp2f(lm - Mall);
            float s0 = 0.f;
            const int hk = h >> 3;
            for (int tk2 = ctx + (lane >> 5); tk2 < LK; tk2 += 2) {
              const unsigned short d = *(const unsigned short*)(vc + ((size_t)bt[tk2 / BS] * BS + (tk2 % BS)) * (HKV * D * 2) + hk * (D * 2) + (u * 32 + dd) * 2);
              s0 += __uint_as_float((unsigned)d << 16);
            }
            s0 += __shfl_xor(s0, 32);
            a0 = a0 * wv + s0 * wm;
            W = W * wv + wm * (float)(LK - ctx);
          }
        }
        const float res = a0 * __builtin_amdgcn_rcpf(W);
        const unsigned pk = cvtpk(res, dpp<0xB1>(res));
        const int oo = (done && lane < 32 && !(lane & 1)) ? (h * D + u * 32 + lane) * 2 : OOB;
        __builtin_amdgcn_raw_buffer_store_b32(pk, ro, oo, 0, 0);
        if (done) {
          if (c == 0 && lane == 0) *(unsigned*)ws = e1_;
          break;
        }
      }
      if (first) {
        TS(9);
        unsigned spins = 0;
        while (__builtin_amdgcn_readfirstlane(ctl[4]) < POLL_START && ++spins < (1u << 22)) __builtin_amdgcn_s_sleep(2);
        TS(10);
        first = 0;
      }
      if (POLL_SLEEP) __builtin_amdgcn_s_sleep(POLL_SLEEP);
      if (++pass > (1u << 20)) return;
    }
    TS(13);
  }
  FLUSH_STAMPS();
}

}

extern "C" void kernel_launch(void* const* d_in, const int* in_sizes, int n_in, void* d_out, int out_size, void* d_ws,
                              size_t ws_size, hipStream_t stream) {
  attn_decode<<<NWG, NTHR, 0, stream>>>((const char*)d_in[0], (const char*)d_in[1], (const char*)d_in[2], (const int*)d_in[3],
                                        (const int*)d_in[4], (char*)d_ws, (char*)d_out, in_sizes[1] * 2, 0);
}
```

```cpp
#include <hip/hip_runtime.h>
#include <hip/hip_bf16.h>
#include <stdint.h>

#define POLL_START 4u
#define DELAY0 0
#define DELAY1 0
#define DRY_OOB 1
#define NO_EXCH 0
#define STR_(x) #x
#define STR(x) STR_(x)
#define STAG 10
#define SCHED 2
#define ROT 1
#define KV_AUX 2

#define TS(i)
#define TS2(i)
#define FLUSH_STAMPS()

namespace {

constexpr int H = 32, HKV = 4, D = 128, BS = 16, NB = 4096, G = H / HKV;
constexpr int LK = NB * BS;
constexpr int NWG = 256, NWAVE = 8, NTHR = NWAVE * 64;
constexpr int UPW = NB / (NWG * 2);
static_assert(UPW == 8, "straight-line body is written for 8 pages per wave");
constexpr float SCALE = 0.08838834764831845f;
constexpr float LOG2E = 1.4426950408889634f;
constexpr float C2 = SCALE * LOG2E;
constexpr int OOB = 0x40000000;
constexpr int WS_VAL = 4096;
constexpr int WS_BYTES = WS_VAL + H * 4 * NWG * 128;
constexpr int AUX_SC1 = 16;

typedef unsigned v4u __attribute__((ext_vector_type(4)));
typedef unsigned v2u __attribute__((ext_vector_type(2)));
typedef float f32x4 __attribute__((ext_vector_type(4)));
typedef __bf16 bf16x8 __attribute__((ext_vector_type(8)));
typedef __bf16 bf16x2 __attribute__((ext_vector_type(2)));
typedef float f32x2 __attribute__((ext_vector_type(2)));
#define LAS __attribute__((address_space(3)))
#define CAS __attribute__((address_space(4)))

__device__ __forceinline__ f32x4 mfma(v4u a, v4u b, f32x4 c) {
  return __builtin_amdgcn_mfma_f32_16x16x32_bf16(__builtin_bit_cast(bf16x8, a), __builtin_bit_cast(bf16x8, b), c, 0, 0, 0);
}
__device__ __forceinline__ unsigned cvtpk(float lo, float hi) {
  f32x2 v = {lo, hi};
  bf16x2 r = __builtin_convertvector(v, bf16x2);
  return __builtin_bit_cast(unsigned, r);
}
template <int CTRL> __device__ __forceinline__ float dpp(float x) {
  return __builtin_bit_cast(float, __builtin_amdgcn_mov_dpp(__builtin_bit_cast(int, x), CTRL, 0xf, 0xf, true));
}
template <int CTRL> __device__ __forceinline__ unsigned dppu(unsigned x) {
  return (unsigned)__builtin_amdgcn_mov_dpp((int)x, CTRL, 0xf, 0xf, true);
}
__device__ __forceinline__ float xg_max(float x) {
  auto s = __builtin_amdgcn_permlane16_swap(__float_as_uint(x), __float_as_uint(x), false, false);
  x = fmaxf(__uint_as_float(s[0]), __uint_as_float(s[1]));
  auto t = __builtin_amdgcn_permlane32_swap(__float_as_uint(x), __float_as_uint(x), false, false);
  return fmaxf(__uint_as_float(t[0]), __uint_as_float(t[1]));
}
__device__ __forceinline__ float xg_sum(float x) {
  auto s = __builtin_amdgcn_permlane16_swap(__float_as_uint(x), __float_as_uint(x), false, false);
  x = __uint_as_float(s[0]) + __uint_as_float(s[1]);
  auto t = __builtin_amdgcn_permlane32_swap(__float_as_uint(x), __float_as_uint(x), false, false);
  return __uint_as_float(t[0]) + __uint_as_float(t[1]);
}
template <int R> __device__ __forceinline__ float rowb1(float x) {
  int t = __builtin_amdgcn_update_dpp(0, __builtin_bit_cast(int, x), 0x150 + R, 0x5, 0xf, false);
  t = __builtin_amdgcn_update_dpp(t, __builtin_bit_cast(int, x), 0x150 + 4 + R, 0xa, 0xf, false);
  return __builtin_bit_cast(float, t);
}
__device__ __forceinline__ f32x4 rowb(float x) { return f32x4{rowb1<0>(x), rowb1<1>(x), rowb1<2>(x), rowb1<3>(x)}; }

struct Page { v4u r[4]; };

__device__ __forceinline__ Page load_k(__amdgpu_buffer_rsrc_t rs, int so, int vo) {
  Page p;
  p.r[0] = __builtin_amdgcn_raw_buffer_load_b128(rs, vo, so, KV_AUX);
  p.r[1] = __builtin_amdgcn_raw_buffer_load_b128(rs, vo + 128, so, KV_AUX);
  p.r[2] = __builtin_amdgcn_raw_buffer_load_b128(rs, vo, so + 8192, KV_AUX);
  p.r[3] = __builtin_amdgcn_raw_buffer_load_b128(rs, vo + 128, so + 8192, KV_AUX);
  return p;
}
__device__ __forceinline__ Page load_v(__amdgpu_buffer_rsrc_t rs, int so, int vo) {
  Page p;
  p.r[0] = __builtin_amdgcn_raw_buffer_load_b128(rs, vo, so, KV_AUX);
  p.r[1] = __builtin_amdgcn_raw_buffer_load_b128(rs, vo + 1024, so, KV_AUX);
  p.r[2] = __builtin_amdgcn_raw_buffer_load_b128(rs, vo + 2048, so, KV_AUX);
  p.r[3] = __builtin_amdgcn_raw_buffer_load_b128(rs, vo + 3072, so, KV_AUX);
  return p;
}

struct QFrag { v4u q[2]; };
__device__ __forceinline__ f32x4 qk_page(const Page& k, const QFrag& q) {
  const f32x4 z = {0.f, 0.f, 0.f, 0.f};
  f32x4 d0 = mfma(k.r[0], q.q[0], z);
  f32x4 d1 = mfma(k.r[2], q.q[0], z);
  d0 = mfma(k.r[1], q.q[1], d0);
  d1 = mfma(k.r[3], q.q[1], d1);
  f32x4 x;
#pragma unroll
  for (int r = 0; r < 4; ++r) {
    auto s = __builtin_amdgcn_permlane32_swap(__float_as_uint(d0[r]), __float_as_uint(d1[r]), false, false);
    x[r] = __uint_as_float(s[0]) + dpp<0x128>(__uint_as_float(s[1]));
  }
  return x;
}

struct State { float m, l, lse; f32x4 acc[8]; };

template <bool FIRST, bool MAYFIN>
__device__ __forceinline__ v4u softmax_step(State& st, f32x4 xa, f32x4 xb, int rema, int remb, int g4, bool fin) {
  if (remb < BS) {
    const int lima = rema - g4, limb = remb - g4;
#pragma unroll
    for (int r = 0; r < 4; ++r) {
      xa[r] = (r < lima) ? xa[r] : -INFINITY;
      xb[r] = (r < limb) ? xb[r] : -INFINITY;
    }
  }
  float mx = fmaxf(fmaxf(fmaxf(xa[0], xa[1]), fmaxf(xa[2], xa[3])), fmaxf(fmaxf(xb[0], xb[1]), fmaxf(xb[2], xb[3])));
  mx = xg_max(mx);
  float mn = FIRST ? mx : fmaxf(st.m, mx);
  const float mc = mn * C2;
  float pa[4], pb[4];
#pragma unroll
  for (int r = 0; r < 4; ++r) {
    pa[r] = __builtin_amdgcn_exp2f(fmaf(xa[r], C2, -mc));
    pb[r] = __builtin_amdgcn_exp2f(fmaf(xb[r], C2, -mc));
  }
  const float ps = ((pa[0] + pa[1]) + (pa[2] + pa[3])) + ((pb[0] + pb[1]) + (pb[2] + pb[3]));
  if (FIRST) {
    st.l = ps;
  } else {
    float alpha = __builtin_amdgcn_exp2f(fmaf(st.m, C2, -mc));
    st.l = fmaf(st.l, alpha, ps);
    if (MAYFIN) {
      const float L = xg_sum(st.l);
      const float inv = fin ? __builtin_amdgcn_rcpf(L) : 1.f;
      st.lse = mc + __builtin_amdgcn_logf(L);
      alpha *= inv;
#pragma unroll
      for (int r = 0; r < 4; ++r) { pa[r] *= inv; pb[r] *= inv; }
    }
    const f32x4 av = rowb(alpha);
#pragma unroll
    for (int m = 0; m < 8; ++m) st.acc[m] *= av;
  }
  st.m = mn;
  v4u p;
  p[0] = cvtpk(pa[0], pa[1]);
  p[1] = cvtpk(pa[2], pa[3]);
  p[2] = cvtpk(pb[0], pb[1]);
  p[3] = cvtpk(pb[2], pb[3]);
  return p;
}

template <bool FIRST>
__device__ __forceinline__ void pv_step(State& st, const Page& va, const Page& vb, v4u p) {
#pragma unroll
  for (int m = 0; m < 8; ++m) {
    const int w = m >> 1;
    const unsigned sel = (m & 1) ? 0x07060302u : 0x05040100u;
    v4u f;
    f[0] = __builtin_amdgcn_perm(va.r[1][w], va.r[0][w], sel);
    f[1] = __builtin_amdgcn_perm(va.r[3][w], va.r[2][w], sel);
    f[2] = __builtin_amdgcn_perm(vb.r[1][w], vb.r[0][w], sel);
    f[3] = __builtin_amdgcn_perm(vb.r[3][w], vb.r[2][w], sel);
    if (FIRST) {
      const f32x4 z = {0.f, 0.f, 0.f, 0.f};
      st.acc[m] = mfma(p, f, z);
    } else {
      st.acc[m] = mfma(p, f, st.acc[m]);
    }
  }
}

constexpr int REC = 8 * 32 * 4 + 16;
constexpr int LDS_CTL = HKV * REC;
constexpr int LDS_FIN = LDS_CTL + 8;
constexpr int FIN_ROW = 36;
constexpr int LDS_WORDS = LDS_FIN + 5 * FIN_ROW;

__device__ __forceinline__ void merge_record(State& st, const float* rec, int lane, int g) {
  f32x4 o[8];
#pragma unroll
  for (int m = 0; m < 8; ++m) o[m] = *(const f32x4*)(rec + (m * 32 + (lane & 31)) * 4);
  const f32x2 ml = *(const f32x2*)(rec + 1024 + 2 * (lane & 7));
  const float M = fmaxf(st.m, ml[0]);
  const float Mc = (M == -INFINITY) ? 0.f : M * C2;
  const float e1 = __builtin_amdgcn_exp2f(fmaf(st.m, C2, -Mc)), e2 = __builtin_amdgcn_exp2f(fmaf(ml[0], C2, -Mc));
  st.l = fmaf(st.l, e1, g == 0 ? e2 * ml[1] : 0.f);
  st.m = M;
  const f32x4 v1 = rowb(e1), v2 = rowb(e2);
#pragma unroll
  for (int m = 0; m < 8; ++m) st.acc[m] = st.acc[m] * v1 + o[m] * v2;
}

#define SB() do { asm volatile("" ::: "memory"); __builtin_amdgcn_sched_barrier(0); } while (0)
#define CB() asm volatile("" ::: "memory")
#define LGKM0() asm volatile("s_waitcnt lgkmcnt(0)" ::: "memory")
#define LDS_ADD(p, v) __hip_atomic_fetch_add((LAS unsigned*)(p), (v), __ATOMIC_RELAXED, __HIP_MEMORY_SCOPE_WORKGROUP)

__global__ void __launch_bounds__(NTHR, 2)
attn_decode(const char* q, const char* kc, const char* vc, const int* bt, const int* ctx_lens,
            char* ws, char* out, int kv_bytes, int pad_) {
  __shared__ __attribute__((aligned(16))) float lds[LDS_WORDS];
  const int tid = threadIdx.x, lane = tid & 63;
  const int w = __builtin_amdgcn_readfirstlane(tid >> 6);
  TS(0);
  const int kvh = w & 3, sub = w >> 2, c = blockIdx.x;
  const int n = lane & 15, g = lane >> 4;
  asm volatile("" :: "s"(q), "s"(kc), "s"(vc), "s"(bt), "s"(ctx_lens), "s"(ws), "s"(out), "s"(kv_bytes));

  int phys[UPW];
  const int cc_ = (c + 64 * kvh) & (NWG - 1);
  const int pc = ROT == 0 ? c : ROT == 1 ? cc_ : ROT == 2 ? ((c + 65 * kvh) & (NWG - 1)) : ROT == 3 ? ((c + 66 * kvh + sub) & (NWG - 1))
               : ROT == 4 ? ((cc_ & 0xC0) | ((cc_ & 7) << 3) | ((cc_ >> 3) & 7)) : (((cc_ & 7) << 5) | (cc_ >> 3));
  const CAS int* btp = (const CAS int*)bt + (unsigned)(sub * NWG + pc);
#pragma unroll
  for (int i = 0; i < UPW; ++i) phys[i] = btp[i * 2 * NWG];
  int ctx = *(const CAS int*)ctx_lens;
  const unsigned ep = *(const CAS unsigned*)ws;
  volatile LAS unsigned* ctl = (volatile LAS unsigned*)(lds + LDS_CTL);
  if (tid < 8) ctl[tid] = 0u;

  QFrag qf;
  const char* qrow = q + (kvh * G + (n & 7)) * (D * 2) + (n >> 3) * 64 + g * 16;
  qf.q[0] = *(const v4u*)(qrow);
  qf.q[1] = *(const v4u*)(qrow + 128);
  const int koff = (n & 7) * 1024 + (n >> 3) * 64 + g * 16;
  const int voff = g * 4096 + n * 16;
  const __amdgpu_buffer_rsrc_t rk = __builtin_amdgcn_make_buffer_rsrc((void*)(kc + kvh * 256), 0, kv_bytes - kvh * 256, 0x00020000);
  const __amdgpu_buffer_rsrc_t rv = __builtin_amdgcn_make_buffer_rsrc((void*)(vc + kvh * 256), 0, kv_bytes - kvh * 256, 0x00020000);

  asm volatile("" :: "s"(phys[0]), "s"(phys[1]), "s"(phys[2]), "s"(phys[3]), "s"(phys[4]), "s"(phys[5]), "s"(phys[6]), "s"(phys[7]),
               "s"(ctx), "s"(ep));
  LGKM0();
  __builtin_amdgcn_s_barrier();
  SB();
  ctx = ctx < 0 ? 0 : (ctx > LK ? LK : ctx);
  int rem[UPW], so[UPW];
#pragma unroll
  for (int i = 0; i < UPW; ++i) {
    rem[i] = ctx - ((i * 2 + sub) * NWG + pc) * BS;
    so[i] = rem[i] > 0 ? (phys[i] & 0xffff) << 14 : OOB;
  }
  const int g4 = 4 * g;

  State st;
  st.m = -INFINITY; st.l = 0.f; st.lse = -INFINITY;
#pragma unroll
  for (int m = 0; m < 8; ++m) st.acc[m] = f32x4{0.f, 0.f, 0.f, 0.f};
  float* rec = lds + kvh * REC;
  int merged = 0;

  TS(1);
#define LK2(A, B, i) Page A = load_k(rk, so[i], koff); Page B = load_k(rk, so[i + 1], koff); SB()
#define LV2(A, B, i) Page A = load_v(rv, so[i], voff); Page B = load_v(rv, so[i + 1], voff); SB()
#define QK2(A, B) xa = qk_page(A, qf); xb = qk_page(B, qf); SB()
#define SM(F, MF, i, fin) p = softmax_step<F, MF>(st, xa, xb, rem[i], rem[i + 1], g4, fin); SB()
#define PV(F, A, B) pv_step<F>(st, A, B, p); SB()
#define TRY_MERGE() do { if (__builtin_amdgcn_readfirstlane(ctl[kvh]) == 3u) { CB(); merge_record(st, rec, lane, g); merged = 1; } SB(); } while (0)
  f32x4 xa, xb; v4u p;
  LK2(K0, K1, 0); LV2(V0, V1, 0);
  TS2(2);
  if (rem[0] > 0) {
    QK2(K0, K1); LK2(K2, K3, 2); SM(true, false, 0, false); PV(true, V0, V1);
    TS2(3);
    LV2(V2, V3, 2);
    if (rem[2] > 0) {
      QK2(K2, K3); LK2(K4, K5, 4); SM(false, false, 2, false); PV(false, V2, V3);
      TS2(4);
      LV2(V4, V5, 4);
      if (rem[6] > 0) {
        QK2(K4, K5); LK2(K6, K7, 6); SM(false, false, 4, false); PV(false, V4, V5);
        LV2(V6, V7, 6);
        TRY_MERGE(); QK2(K6, K7); SM(false, true, 6, merged != 0); PV(false, V6, V7);
      } else if (rem[4] > 0) {
        TRY_MERGE(); QK2(K4, K5); SM(false, true, 4, merged != 0);
        TS2(5);
        PV(false, V4, V5);
      }
    }
  }
  TS2(6);
  const unsigned e1_ = ep + 1u, hA = e1_ * 0x9E3779B1u, KX = (e1_ << 1) | 1u, hB = hA ^ KX;
  const __amdgpu_buffer_rsrc_t rw = __builtin_amdgcn_make_buffer_rsrc((void*)ws, 0, WS_BYTES, 0x00020000);
  unsigned t = 1;
  if (!merged) {
    t = 0;
    if (lane == 0) t = LDS_ADD(ctl + kvh, 1u);
    t = __builtin_amdgcn_readfirstlane(t);
    if (t != 0) {
      while (t != 3u && t != 4u) { __builtin_amdgcn_s_sleep(1); t = __builtin_amdgcn_readfirstlane(ctl[kvh]); }
      CB();
      merge_record(st, rec, lane, g);
      const float L = xg_sum(st.l);
      const f32x4 iv = rowb((L > 0.f) ? __builtin_amdgcn_rcpf(L) : 0.f);
      st.lse = (L > 0.f) ? fmaf(st.m, C2, __builtin_amdgcn_logf(L)) : -INFINITY;
#pragma unroll
      for (int m = 0; m < 8; ++m) st.acc[m] *= iv;
    }
  }
  if (t != 0) {
    const f32x4 lb = rowb(st.lse);
    const int vo = WS_VAL + ((((kvh * G + 4 * (g & 1)) * 4 + (n >> 2)) * NWG + c) * 8 + 2 * (n & 3) + (g >> 1)) * 16;
#pragma unroll
    for (int r = 0; r < 4; ++r) {
      const unsigned w0 = cvtpk(st.acc[0][r], st.acc[1][r]), w1 = cvtpk(st.acc[2][r], st.acc[3][r]);
      const unsigned w2 = cvtpk(st.acc[4][r], st.acc[5][r]), w3 = cvtpk(st.acc[6][r], st.acc[7][r]);
      const auto s0 = __builtin_amdgcn_permlane32_swap(w0, w2, false, false);
      const auto s1 = __builtin_amdgcn_permlane32_swap(w1, w3, false, false);
      const unsigned l = __float_as_uint(lb[r]);
      const v4u gr = {s0[0], l ^ hA, s1[0], l ^ hB};
      __builtin_amdgcn_raw_buffer_store_b128(gr, rw, (NO_EXCH ? OOB : vo) + r * (4 * NWG * 128), 0, AUX_SC1);
    }
    if (lane == 0) LDS_ADD(ctl + 4, 1u);
    TS(7);
  } else {
    const float lsum = xg_sum(st.l);
    if (lane < 32) {
#pragma unroll
      for (int m = 0; m < 8; ++m) *(f32x4*)(rec + (m * 32 + lane) * 4) = st.acc[m];
      if (lane < G) *(f32x2*)(rec + 1024 + 2 * lane) = f32x2{st.m, lsum};
    }
    LGKM0();
    if (lane == 0) LDS_ADD(ctl + kvh, 2u);
    TS2(8);
    if (NO_EXCH) { FLUSH_STAMPS(); return; }
    const __amdgpu_buffer_rsrc_t ro = __builtin_amdgcn_make_buffer_rsrc((void*)out, 0, H * D * 2, 0x00020000);
    const int h = c >> 3, j = c & 7;
    const int vo = WS_VAL + ((c >> 1) * NWG + 64 * kvh + (lane >> 2)) * 128 + (c & 1) * 64 + (lane & 3) * 16;
    const v4u rdesc = {(unsigned)(uintptr_t)ws, (unsigned)((uintptr_t)ws >> 32), (unsigned)WS_BYTES, 0x00020000u};
    v4u ra[4], rb[4];
#define KEEP_RING() asm volatile("" :: "{v[128:131]}"(ra[0]), "{v[132:135]}"(ra[1]), "{v[136:139]}"(ra[2]), "{v[140:143]}"(ra[3]), "{v[144:147]}"(rb[0]), "{v[148:151]}"(rb[1]), "{v[152:155]}"(rb[2]), "{v[156:159]}"(rb[3]))
    unsigned pass = 0;
    int dry = __builtin_amdgcn_readfirstlane(ctl[4]) == 0u;
#pragma clang loop unroll(disable)
    for (;;) {
      { int fv = dry; asm volatile("" : "+v"(fv)); dry = __builtin_amdgcn_readfirstlane(fv); }
      const int vod = dry ? OOB : vo;
      unsigned cnt = dry ? 1u : (1u << 20);
      unsigned gt[4], gu[4], gw[4];
      unsigned long long m_ma[4], m_mb[4], m_c[4], mx0, mx1;
      asm volatile(
          "s_nop 4\n\t"
          "s_mov_b64 %[mb0], -1\n\t"
          "s_mov_b64 %[mb1], -1\n\t"
          "s_mov_b64 %[mb2], -1\n\t"
          "s_mov_b64 %[mb3], -1\n\t"
          "buffer_load_dwordx4 v[128:131], %[vo], %[rs], 0 offen sc1\n\t"
          "buffer_load_dwordx4 v[132:135], %[vo], %[rs], 0 offen offset:2048 sc1\n\t"
          "buffer_load_dwordx4 v[136:139], %[vo], %[rs], %[s4k] offen sc1\n\t"
          "buffer_load_dwordx4 v[140:143], %[vo], %[rs], %[s4k] offen offset:2048 sc1\n\t"
          "s_sleep " STR(STAG) "\n\t"
          "buffer_load_dwordx4 v[144:147], %[vo], %[rs], 0 offen sc1\n\t"
          "buffer_load_dwordx4 v[148:151], %[vo], %[rs], 0 offen offset:2048 sc1\n\t"
          "buffer_load_dwordx4 v[152:155], %[vo], %[rs], %[s4k] offen sc1\n\t"
          "buffer_load_dwordx4 v[156:159], %[vo], %[rs], %[s4k] offen offset:2048 sc1\n\t"
          "1:\n\t"
          "s_waitcnt vmcnt(4)\n\t"
          "v_xor_b32 %[t0], v129, v131\n\t"
          "v_xor_b32 %[t1], v133, v135\n\t"
          "v_xor_b32 %[t2], v137, v139\n\t"
          "v_xor_b32 %[t3], v141, v143\n\t"
          "v_xor_b32_dpp %[u0], v129, v129 quad_perm:[1,0,3,2] row_mask:0xf bank_mask:0xf\n\t"
          "v_xor_b32_dpp %[u1], v133, v133 quad_perm:[1,0,3,2] row_mask:0xf bank_mask:0xf\n\t"
          "v_xor_b32_dpp %[u2], v137, v137 quad_perm:[1,0,3,2] row_mask:0xf bank_mask:0xf\n\t"
          "v_xor_b32_dpp %[u3], v141, v141 quad_perm:[1,0,3,2] row_mask:0xf bank_mask:0xf\n\t"
          "v_bitop3_b32 %[t0], %[t0], %[kx], %[u0] bitop3:0xbe\n\t"
          "v_bitop3_b32 %[t1], %[t1], %[kx], %[u1] bitop3:0xbe\n\t"
          "v_bitop3_b32 %[t2], %[t2], %[kx], %[u2] bitop3:0xbe\n\t"
          "v_bitop3_b32 %[t3], %[t3], %[kx], %[u3] bitop3:0xbe\n\t"
          "v_cmp_ne_u32 %[ma0], 0, %[t0]\n\t"
          "v_cmp_ne_u32 %[ma1], 0, %[t1]\n\t"
          "v_cmp_ne_u32 %[ma2], 0, %[t2]\n\t"
          "v_cmp_ne_u32 %[ma3], 0, %[t3]\n\t"
          "s_and_b64 %[c0], %[ma0], %[mb0]\n\t"
          "s_and_b64 %[c1], %[ma1], %[mb1]\n\t"
          "s_and_b64 %[c2], %[ma2], %[mb2]\n\t"
          "s_and_b64 %[c3], %[ma3], %[mb3]\n\t"
          "s_or_b64 %[x0], %[c0], %[c1]\n\t"
          "s_or_b64 %[x1], %[c2], %[c3]\n\t"
          "s_or_b64 %[x0], %[x0], %[x1]\n\t"
          "s_cbranch_scc0 2f\n\t"
          "s_mov_b64 exec, %[c0]\n\t"
          "buffer_load_dwordx4 v[128:131], %[vo], %[rs], 0 offen sc1\n\t"
          "s_mov_b64 exec, %[c1]\n\t"
          "buffer_load_dwordx4 v[132:135], %[vo], %[rs], 0 offen offset:2048 sc1\n\t"
          "s_mov_b64 exec, %[c2]\n\t"
          "buffer_load_dwordx4 v[136:139], %[vo], %[rs], %[s4k] offen sc1\n\t"
          "s_mov_b64 exec, %[c3]\n\t"
          "buffer_load_dwordx4 v[140:143], %[vo], %[rs], %[s4k] offen offset:2048 sc1\n\t"
          "s_mov_b64 exec, -1\n\t"
          "s_waitcnt vmcnt(4)\n\t"
          "v_xor_b32 %[t0], v145, v147\n\t"
          "v_xor_b32 %[t1], v149, v151\n\t"
          "v_xor_b32 %[t2], v153, v155\n\t"
          "v_xor_b32 %[t3], v157, v159\n\t"
          "v_xor_b32_dpp %[u0], v145, v145 quad_perm:[1,0,3,2] row_mask:0xf bank_mask:0xf\n\t"
          "v_xor_b32_dpp %[u1], v149, v149 quad_perm:[1,0,3,2] row_mask:0xf bank_mask:0xf\n\t"
          "v_xor_b32_dpp %[u2], v153, v153 quad_perm:[1,0,3,2] row_mask:0xf bank_mask:0xf\n\t"
          "v_xor_b32_dpp %[u3], v157, v157 quad_perm:[1,0,3,2] row_mask:0xf bank_mask:0xf\n\t"
          "v_bitop3_b32 %[t0], %[t0], %[kx], %[u0] bitop3:0xbe\n\t"
          "v_bitop3_b32 %[t1], %[t1], %[kx], %[u1] bitop3:0xbe\n\t"
          "v_bitop3_b32 %[t2], %[t2], %[kx], %[u2] bitop3:0xbe\n\t"
          "v_bitop3_b32 %[t3], %[t3], %[kx], %[u3] bitop3:0xbe\n\t"
          "v_cmp_ne_u32 %[mb0], 0, %[t0]\n\t"
          "v_cmp_ne_u32 %[mb1], 0, %[t1]\n\t"
          "v_cmp_ne_u32 %[mb2], 0, %[t2]\n\t"
          "v_cmp_ne_u32 %[mb3], 0, %[t3]\n\t"
          "s_and_b64 %[c0], %[ma0], %[mb0]\n\t"
          "s_and_b64 %[c1], %[ma1], %[mb1]\n\t"
          "s_and_b64 %[c2], %[ma2], %[mb2]\n\t"
          "s_and_b64 %[c3], %[ma3], %[mb3]\n\t"
          "s_or_b64 %[x0], %[c0], %[c1]\n\t"
          "s_or_b64 %[x1], %[c2], %[c3]\n\t"
          "s_or_b64 %[x0], %[x0], %[x1]\n\t"
          "s_cbranch_scc0 2f\n\t"
          "s_mov_b64 exec, %[c0]\n\t"
          "buffer_load_dwordx4 v[144:147], %[vo], %[rs], 0 offen sc1\n\t"
          "s_mov_b64 exec, %[c1]\n\t"
          "buffer_load_dwordx4 v[148:151], %[vo], %[rs], 0 offen offset:2048 sc1\n\t"
          "s_mov_b64 exec, %[c2]\n\t"
          "buffer_load_dwordx4 v[152:155], %[vo], %[rs], %[s4k] offen sc1\n\t"
          "s_mov_b64 exec, %[c3]\n\t"
          "buffer_load_dwordx4 v[156:159], %[vo], %[rs], %[s4k] offen offset:2048 sc1\n\t"
          "s_mov_b64 exec, -1\n\t"
          "s_sub_u32 %[cnt], %[cnt], 1\n\t"
          "s_cmp_lg_u32 %[cnt], 0\n\t"
          "s_cbranch_scc1 1b\n\t"
          "2:\n\t"
          "v_cndmask_b32 %[t0], v129, v145, %[ma0]\n\t"
          "v_cndmask_b32 %[u0], v128, v144, %[ma0]\n\t"
          "v_cndmask_b32 %[w0], v130, v146, %[ma0]\n\t"
          "v_cndmask_b32 %[t1], v133, v149, %[ma1]\n\t"
          "v_cndmask_b32 %[u1], v132, v148, %[ma1]\n\t"
          "v_cndmask_b32 %[w1], v134, v150, %[ma1]\n\t"
          "v_cndmask_b32 %[t2], v137, v153, %[ma2]\n\t"
          "v_cndmask_b32 %[u2], v136, v152, %[ma2]\n\t"
          "v_cndmask_b32 %[w2], v138, v154, %[ma2]\n\t"
          "v_cndmask_b32 %[t3], v141, v157, %[ma3]\n\t"
          "v_cndmask_b32 %[u3], v140, v156, %[ma3]\n\t"
          "v_cndmask_b32 %[w3], v142, v158, %[ma3]\n\t"
          "s_nop 1\n\t"
          : "={v[128:131]}"(ra[0]), "={v[132:135]}"(ra[1]), "={v[136:139]}"(ra[2]), "={v[140:143]}"(ra[3]), "={v[144:147]}"(rb[0]), "={v[148:151]}"(rb[1]), "={v[152:155]}"(rb[2]), "={v[156:159]}"(rb[3]), [t0] "=&v"(gt[0]), [t1] "=&v"(gt[1]), [t2] "=&v"(gt[2]), [t3] "=&v"(gt[3]), [u0] "=&v"(gu[0]), [u1] "=&v"(gu[1]), [u2] "=&v"(gu[2]), [u3] "=&v"(gu[3]), [w0] "=&v"(gw[0]), [w1] "=&v"(gw[1]), [w2] "=&v"(gw[2]), [w3] "=&v"(gw[3]), [ma0] "=&s"(m_ma[0]), [ma1] "=&s"(m_ma[1]), [ma2] "=&s"(m_ma[2]), [ma3] "=&s"(m_ma[3]), [mb0] "=&s"(m_mb[0]), [mb1] "=&s"(m_mb[1]), [mb2] "=&s"(m_mb[2]), [mb3] "=&s"(m_mb[3]), [c0] "=&s"(m_c[0]), [c1] "=&s"(m_c[1]), [c2] "=&s"(m_c[2]), [c3] "=&s"(m_c[3]), [x0] "=&s"(mx0), [x1] "=&s"(mx1), [cnt] "+s"(cnt)
          : [vo] "v"(vod), [rs] "s"(rdesc), [s4k] "s"(4096), [kx] "s"(KX)
          : "memory", "scc");
      if (!dry && cnt == 0u) { asm volatile("s_waitcnt vmcnt(0)" ::: "memory"); KEEP_RING(); return; }
      {
        float gl[4];
#pragma unroll
        for (int i = 0; i < 4; ++i) gl[i] = __uint_as_float(gt[i] ^ hA);
        float M = fmaxf(fmaxf(gl[0], gl[1]), fmaxf(gl[2], gl[3]));
        M = fmaxf(M, dpp<0x124>(M)); M = fmaxf(M, dpp<0x128>(M));
        M = xg_max(M);
        const float Ms = (M == -INFINITY) ? 0.f : M;
        float W = 0.f, a0 = 0.f, a1 = 0.f, a2 = 0.f, a3 = 0.f;
#pragma unroll
        for (int i = 0; i < 4; ++i) {
          const float e = __builtin_amdgcn_exp2f(gl[i] - Ms);
          W += e;
          a0 = fmaf(e, __uint_as_float(gu[i] << 16), a0); a1 = fmaf(e, __uint_as_float(gu[i] & 0xffff0000u), a1);
          a2 = fmaf(e, __uint_as_float(gw[i] << 16), a2); a3 = fmaf(e, __uint_as_float(gw[i] & 0xffff0000u), a3);
        }
        const auto s01 = __builtin_amdgcn_permlane32_swap(__float_as_uint(a0), __float_as_uint(a1), false, false);
        const auto s23 = __builtin_amdgcn_permlane32_swap(__float_as_uint(a2), __float_as_uint(a3), false, false);
        const float u01 = __uint_as_float(s01[0]) + __uint_as_float(s01[1]);
        const float u23 = __uint_as_float(s23[0]) + __uint_as_float(s23[1]);
        const auto sx = __builtin_amdgcn_permlane16_swap(__float_as_uint(u01), __float_as_uint(u23), false, false);
        float v = __uint_as_float(sx[0]) + __uint_as_float(sx[1]);
        v += dpp<0x124>(v); v += dpp<0x128>(v);
        W += dpp<0x124>(W); W += dpp<0x128>(W);
        W = xg_sum(W);
        float* slot = lds + LDS_FIN + (dry ? 4 : kvh) * FIN_ROW;
        unsigned tk = 0;
        if (n < 4) {
          slot[4 * n + 2 * (g & 1) + (g >> 1)] = v;
          if (lane == 0) {
            *(f32x2*)(slot + 32) = f32x2{M, W};
            CB();
            int ci = dry ? 6 : 5;
            asm volatile("" : "+v"(ci));
            tk = LDS_ADD(ctl + ci, 1u);
          }
        }
        CB();
        const float* fin = lds + LDS_FIN;
        float mw[4], ww[4], av[4];
#pragma unroll
        for (int i = 0; i < 4; ++i) {
          const f32x2 x = *(const f32x2*)(fin + i * FIN_ROW + 32);
          mw[i] = x[0]; ww[i] = x[1]; av[i] = fin[i * FIN_ROW + n];
        }
        CB();
        tk = __builtin_amdgcn_readfirstlane(tk);
        if (!dry && tk != 3u) {
          TS(11);
          FLUSH_STAMPS();
          KEEP_RING();
          return;
        }
        M = fmaxf(fmaxf(mw[0], mw[1]), fmaxf(mw[2], mw[3]));
        const float Mf = (M == -INFINITY) ? 0.f : M;
        float Wt = 0.f, at = 0.f;
#pragma unroll
        for (int i = 0; i < 4; ++i) {
          const float fi = __builtin_amdgcn_exp2f(mw[i] - Mf);
          Wt = fmaf(fi, ww[i], Wt); at = fmaf(fi, av[i], at);
        }
        if (!dry && ctx < LK && !(M > -1.0e9f * LOG2E + 2048.f)) {
          const float lm = -1.0e9f * LOG2E;
          const float Mall = fmaxf(M, lm);
          const float wv = (M == -INFINITY) ? 0.f : __builtin_amdgcn_exp2f(M - Mall);
          const float wm = __builtin_amdgcn_exp2f(lm - Mall);
          float s0 = 0.f;
          const int hk = h >> 3;
          for (int tk2 = ctx + g; tk2 < LK; tk2 += 4) {
            const unsigned short d = *(const unsigned short*)(vc + ((size_t)bt[tk2 / BS] * BS + (tk2 % BS)) * (HKV * D * 2) + hk * (D * 2) + (j * 16 + n) * 2);
            s0 += __uint_as_float((unsigned)d << 16);
          }
          s0 = xg_sum(s0);
          at = at * wv + s0 * wm;
          Wt = Wt * wv + wm * (float)(LK - ctx);
        }
        const float res = at * __builtin_amdgcn_rcpf(Wt);
        const unsigned pk = cvtpk(res, dpp<0xB1>(res));
        const int oo = (!dry && lane < 16 && !(lane & 1)) ? (h * D + j * 16 + lane) * 2 : OOB;
        __builtin_amdgcn_raw_buffer_store_b32(pk, ro, oo, 0, 0);
        if (!dry) {
          if (c == 0 && lane == 0) *(unsigned*)ws = e1_;
          KEEP_RING();
          pass = (1u << 20) - cnt;
          break;
        }
      }
      asm volatile("s_waitcnt vmcnt(0)" ::: "memory");
      KEEP_RING();
      dry = 0;
      TS2(9);
      unsigned spins = 0;
      while (__builtin_amdgcn_readfirstlane(ctl[4]) < POLL_START && ++spins < (1u << 22)) __builtin_amdgcn_s_sleep(2);
      TS(10);
    }
    TS(13);
  }
  FLUSH_STAMPS();
}

}

extern "C" void kernel_launch(void* const* d_in, const int* in_sizes, int n_in, void* d_out, int out_size, void* d_ws,
                              size_t ws_size, hipStream_t stream) {
  attn_decode<<<NWG, NTHR, 0, stream>>>((const char*)d_in[0], (const char*)d_in[1], (const char*)d_in[2], (const int*)d_in[3],
                                        (const int*)d_in[4], (char*)d_ws, (char*)d_out, in_sizes[1] * 2, 0);
}
```

```cpp
#include <hip/hip_runtime.h>
#include <hip/hip_bf16.h>
#include <stdint.h>

#define POLL_START 4u
#define DELAY0 0
#define DELAY1 0
#define DRY_OOB 1
#define NO_EXCH 0
#define STR_(x) #x
#define STR(x) STR_(x)
#define STAG 10
#define SCHED 2
#define ROT 1
#define CODE_PF 14336
#define KV_AUX 2

#define TS(i)
#define TS2(i)
#define FLUSH_STAMPS()

namespace {

constexpr int H = 32, HKV = 4, D = 128, BS = 16, NB = 4096, G = H / HKV;
constexpr int LK = NB * BS;
constexpr int NWG = 256, NWAVE = 8, NTHR = NWAVE * 64;
constexpr int UPW = NB / (NWG * 2);
static_assert(UPW == 8, "straight-line body is written for 8 pages per wave");
constexpr float SCALE = 0.08838834764831845f;
constexpr float LOG2E = 1.4426950408889634f;
constexpr float C2 = SCALE * LOG2E;
constexpr int OOB = 0x40000000;
constexpr int WS_VAL = 4096;
constexpr int WS_BYTES = WS_VAL + H * 4 * NWG * 128;
constexpr int AUX_SC1 = 16;

typedef unsigned v4u __attribute__((ext_vector_type(4)));
typedef unsigned v2u __attribute__((ext_vector_type(2)));
typedef float f32x4 __attribute__((ext_vector_type(4)));
typedef __bf16 bf16x8 __attribute__((ext_vector_type(8)));
typedef __bf16 bf16x2 __attribute__((ext_vector_type(2)));
typedef float f32x2 __attribute__((ext_vector_type(2)));
#define LAS __attribute__((address_space(3)))
#define CAS __attribute__((address_space(4)))

__device__ __forceinline__ f32x4 mfma(v4u a, v4u b, f32x4 c) {
  return __builtin_amdgcn_mfma_f32_16x16x32_bf16(__builtin_bit_cast(bf16x8, a), __builtin_bit_cast(bf16x8, b), c, 0, 0, 0);
}
__device__ __forceinline__ unsigned cvtpk(float lo, float hi) {
  f32x2 v = {lo, hi};
  bf16x2 r = __builtin_convertvector(v, bf16x2);
  return __builtin_bit_cast(unsigned, r);
}
template <int CTRL> __device__ __forceinline__ float dpp(float x) {
  return __builtin_bit_cast(float, __builtin_amdgcn_mov_dpp(__builtin_bit_cast(int, x), CTRL, 0xf, 0xf, true));
}
template <int CTRL> __device__ __forceinline__ unsigned dppu(unsigned x) {
  return (unsigned)__builtin_amdgcn_mov_dpp((int)x, CTRL, 0xf, 0xf, true);
}
__device__ __forceinline__ float xg_max(float x) {
  auto s = __builtin_amdgcn_permlane16_swap(__float_as_uint(x), __float_as_uint(x), false, false);
  x = fmaxf(__uint_as_float(s[0]), __uint_as_float(s[1]));
  auto t = __builtin_amdgcn_permlane32_swap(__float_as_uint(x), __float_as_uint(x), false, false);
  return fmaxf(__uint_as_float(t[0]), __uint_as_float(t[1]));
}
__device__ __forceinline__ float xg_sum(float x) {
  auto s = __builtin_amdgcn_permlane16_swap(__float_as_uint(x), __float_as_uint(x), false, false);
  x = __uint_as_float(s[0]) + __uint_as_float(s[1]);
  auto t = __builtin_amdgcn_permlane32_swap(__float_as_uint(x), __float_as_uint(x), false, false);
  return __uint_as_float(t[0]) + __uint_as_float(t[1]);
}
template <int R> __device__ __forceinline__ float rowb1(float x) {
  int t = __builtin_amdgcn_update_dpp(0, __builtin_bit_cast(int, x), 0x150 + R, 0x5, 0xf, false);
  t = __builtin_amdgcn_update_dpp(t, __builtin_bit_cast(int, x), 0x150 + 4 + R, 0xa, 0xf, false);
  return __builtin_bit_cast(float, t);
}
__device__ __forceinline__ f32x4 rowb(float x) { return f32x4{rowb1<0>(x), rowb1<1>(x), rowb1<2>(x), rowb1<3>(x)}; }

struct Page { v4u r[4]; };

__device__ __forceinline__ Page load_k(__amdgpu_buffer_rsrc_t rs, int so, int vo) {
  Page p;
  p.r[0] = __builtin_amdgcn_raw_buffer_load_b128(rs, vo, so, KV_AUX);
  p.r[1] = __builtin_amdgcn_raw_buffer_load_b128(rs, vo + 128, so, KV_AUX);
  p.r[2] = __builtin_amdgcn_raw_buffer_load_b128(rs, vo, so + 8192, KV_AUX);
  p.r[3] = __builtin_amdgcn_raw_buffer_load_b128(rs, vo + 128, so + 8192, KV_AUX);
  return p;
}
__device__ __forceinline__ Page load_v(__amdgpu_buffer_rsrc_t rs, int so, int vo) {
  Page p;
  p.r[0] = __builtin_amdgcn_raw_buffer_load_b128(rs, vo, so, KV_AUX);
  p.r[1] = __builtin_amdgcn_raw_buffer_load_b128(rs, vo + 1024, so, KV_AUX);
  p.r[2] = __builtin_amdgcn_raw_buffer_load_b128(rs, vo + 2048, so, KV_AUX);
  p.r[3] = __builtin_amdgcn_raw_buffer_load_b128(rs, vo + 3072, so, KV_AUX);
  return p;
}

struct QFrag { v4u q[2]; };
__device__ __forceinline__ f32x4 qk_page(const Page& k, const QFrag& q) {
  const f32x4 z = {0.f, 0.f, 0.f, 0.f};
  f32x4 d0 = mfma(k.r[0], q.q[0], z);
  f32x4 d1 = mfma(k.r[2], q.q[0], z);
  d0 = mfma(k.r[1], q.q[1], d0);
  d1 = mfma(k.r[3], q.q[1], d1);
  f32x4 x;
#pragma unroll
  for (int r = 0; r < 4; ++r) {
    auto s = __builtin_amdgcn_permlane32_swap(__float_as_uint(d0[r]), __float_as_uint(d1[r]), false, false);
    x[r] = __uint_as_float(s[0]) + dpp<0x128>(__uint_as_float(s[1]));
  }
  return x;
}

struct State { float m, l, lse; f32x4 acc[8]; };

template <bool FIRST, bool MAYFIN>
__device__ __forceinline__ v4u softmax_step(State& st, f32x4 xa, f32x4 xb, int rema, int remb, int g4, bool fin) {
  if (remb < BS) {
    const int lima = rema - g4, limb = remb - g4;
#pragma unroll
    for (int r = 0; r < 4; ++r) {
      xa[r] = (r < lima) ? xa[r] : -INFINITY;
      xb[r] = (r < limb) ? xb[r] : -INFINITY;
    }
  }
  float mx = fmaxf(fmaxf(fmaxf(xa[0], xa[1]), fmaxf(xa[2], xa[3])), fmaxf(fmaxf(xb[0], xb[1]), fmaxf(xb[2], xb[3])));
  mx = xg_max(mx);
  float mn = FIRST ? mx : fmaxf(st.m, mx);
  const float mc = mn * C2;
  float pa[4], pb[4];
#pragma unroll
  for (int r = 0; r < 4; ++r) {
    pa[r] = __builtin_amdgcn_exp2f(fmaf(xa[r], C2, -mc));
    pb[r] = __builtin_amdgcn_exp2f(fmaf(xb[r], C2, -mc));
  }
  const float ps = ((pa[0] + pa[1]) + (pa[2] + pa[3])) + ((pb[0] + pb[1]) + (pb[2] + pb[3]));
  if (FIRST) {
    st.l = ps;
  } else {
    float alpha = __builtin_amdgcn_exp2f(fmaf(st.m, C2, -mc));
    st.l = fmaf(st.l, alpha, ps);
    if (MAYFIN) {
      const float L = xg_sum(st.l);
      const float inv = fin ? __builtin_amdgcn_rcpf(L) : 1.f;
      st.lse = mc + __builtin_amdgcn_logf(L);
      alpha *= inv;
#pragma unroll
      for (int r = 0; r < 4; ++r) { pa[r] *= inv; pb[r] *= inv; }
    }
    const f32x4 av = rowb(alpha);
#pragma unroll
    for (int m = 0; m < 8; ++m) st.acc[m] *= av;
  }
  st.m = mn;
  v4u p;
  p[0] = cvtpk(pa[0], pa[1]);
  p[1] = cvtpk(pa[2], pa[3]);
  p[2] = cvtpk(pb[0], pb[1]);
  p[3] = cvtpk(pb[2], pb[3]);
  return p;
}

template <bool FIRST>
__device__ __forceinline__ void pv_step(State& st, const Page& va, const Page& vb, v4u p) {
#pragma unroll
  for (int m = 0; m < 8; ++m) {
    const int w = m >> 1;
    const unsigned sel = (m & 1) ? 0x07060302u : 0x05040100u;
    v4u f;
    f[0] = __builtin_amdgcn_perm(va.r[1][w], va.r[0][w], sel);
    f[1] = __builtin_amdgcn_perm(va.r[3][w], va.r[2][w], sel);
    f[2] = __builtin_amdgcn_perm(vb.r[1][w], vb.r[0][w], sel);
    f[3] = __builtin_amdgcn_perm(vb.r[3][w], vb.r[2][w], sel);
    if (FIRST) {
      const f32x4 z = {0.f, 0.f, 0.f, 0.f};
      st.acc[m] = mfma(p, f, z);
    } else {
      st.acc[m] = mfma(p, f, st.acc[m]);
    }
  }
}

constexpr int REC = 8 * 32 * 4 + 16;
constexpr int LDS_CTL = HKV * REC;
constexpr int LDS_FIN = LDS_CTL + 8;
constexpr int FIN_ROW = 36;
constexpr int LDS_WORDS = LDS_FIN + 5 * FIN_ROW;

__device__ __forceinline__ void merge_record(State& st, const float* rec, int lane, int g) {
  f32x4 o[8];
#pragma unroll
  for (int m = 0; m < 8; ++m) o[m] = *(const f32x4*)(rec + (m * 32 + (lane & 31)) * 4);
  const f32x2 ml = *(const f32x2*)(rec + 1024 + 2 * (lane & 7));
  const float M = fmaxf(st.m, ml[0]);
  const float Mc = (M == -INFINITY) ? 0.f : M * C2;
  const float e1 = __builtin_amdgcn_exp2f(fmaf(st.m, C2, -Mc)), e2 = __builtin_amdgcn_exp2f(fmaf(ml[0], C2, -Mc));
  st.l = fmaf(st.l, e1, g == 0 ? e2 * ml[1] : 0.f);
  st.m = M;
  const f32x4 v1 = rowb(e1), v2 = rowb(e2);
#pragma unroll
  for (int m = 0; m < 8; ++m) st.acc[m] = st.acc[m] * v1 + o[m] * v2;
}

#define SB() do { asm volatile("" ::: "memory"); __builtin_amdgcn_sched_barrier(0); } while (0)
#define CB() asm volatile("" ::: "memory")
#define LGKM0() asm volatile("s_waitcnt lgkmcnt(0)" ::: "memory")
#define LDS_ADD(p, v) __hip_atomic_fetch_add((LAS unsigned*)(p), (v), __ATOMIC_RELAXED, __HIP_MEMORY_SCOPE_WORKGROUP)

__global__ void __launch_bounds__(NTHR, 2)
attn_decode(const char* q, const char* kc, const char* vc, const int* bt, const int* ctx_lens,
            char* ws, char* out, int kv_bytes, int pad_) {
  __shared__ __attribute__((aligned(16))) float lds[LDS_WORDS];
  const int tid = threadIdx.x, lane = tid & 63;
  const int w = __builtin_amdgcn_readfirstlane(tid >> 6);
  unsigned cpf = 0;
  {
    const __amdgpu_buffer_rsrc_t rc = __builtin_amdgcn_make_buffer_rsrc((void*)(__builtin_amdgcn_s_getpc() & ~127ull), 0, CODE_PF, 0x00020000);
    cpf = __builtin_amdgcn_raw_buffer_load_b32(rc, tid * 128, 0, 0);
    asm volatile("" ::: "memory");
  }
  TS(0);
  const int kvh = w & 3, sub = w >> 2, c = blockIdx.x;
  const int n = lane & 15, g = lane >> 4;
  asm volatile("" :: "s"(q), "s"(kc), "s"(vc), "s"(bt), "s"(ctx_lens), "s"(ws), "s"(out), "s"(kv_bytes));

  int phys[UPW];
  const int cc_ = (c + 64 * kvh) & (NWG - 1);
  const int pc = ROT == 0 ? c : ROT == 1 ? cc_ : ROT == 2 ? ((c + 65 * kvh) & (NWG - 1)) : ROT == 3 ? ((c + 66 * kvh + sub) & (NWG - 1))
               : ROT == 4 ? ((cc_ & 0xC0) | ((cc_ & 7) << 3) | ((cc_ >> 3) & 7)) : (((cc_ & 7) << 5) | (cc_ >> 3));
  const CAS int* btp = (const CAS int*)bt + (unsigned)(sub * NWG + pc);
#pragma unroll
  for (int i = 0; i < UPW; ++i) phys[i] = btp[i * 2 * NWG];
  int ctx = *(const CAS int*)ctx_lens;
  const unsigned ep = *(const CAS unsigned*)ws;
  volatile LAS unsigned* ctl = (volatile LAS unsigned*)(lds + LDS_CTL);
  if (tid < 8) ctl[tid] = 0u;

  QFrag qf;
  const char* qrow = q + (kvh * G + (n & 7)) * (D * 2) + (n >> 3) * 64 + g * 16;
  qf.q[0] = *(const v4u*)(qrow);
  qf.q[1] = *(const v4u*)(qrow + 128);
  const int koff = (n & 7) * 1024 + (n >> 3) * 64 + g * 16;
  const int voff = g * 4096 + n * 16;
  const __amdgpu_buffer_rsrc_t rk = __builtin_amdgcn_make_buffer_rsrc((void*)(kc + kvh * 256), 0, kv_bytes - kvh * 256, 0x00020000);
  const __amdgpu_buffer_rsrc_t rv = __builtin_amdgcn_make_buffer_rsrc((void*)(vc + kvh * 256), 0, kv_bytes - kvh * 256, 0x00020000);

  asm volatile("" :: "s"(phys[0]), "s"(phys[1]), "s"(phys[2]), "s"(phys[3]), "s"(phys[4]), "s"(phys[5]), "s"(phys[6]), "s"(phys[7]),
               "s"(ctx), "s"(ep));
  LGKM0();
  __builtin_amdgcn_s_barrier();
  SB();
  ctx = ctx < 0 ? 0 : (ctx > LK ? LK : ctx);
  int rem[UPW], so[UPW];
#pragma unroll
  for (int i = 0; i < UPW; ++i) {
    rem[i] = ctx - ((i * 2 + sub) * NWG + pc) * BS;
    so[i] = rem[i] > 0 ? (phys[i] & 0xffff) << 14 : OOB;
  }
  const int g4 = 4 * g;

  State st;
  st.m = -INFINITY; st.l = 0.f; st.lse = -INFINITY;
#pragma unroll
  for (int m = 0; m < 8; ++m) st.acc[m] = f32x4{0.f, 0.f, 0.f, 0.f};
  float* rec = lds + kvh * REC;
  int merged = 0;

  TS(1);
#define LK2(A, B, i) Page A = load_k(rk, so[i], koff); Page B = load_k(rk, so[i + 1], koff); SB()
#define LV2(A, B, i) Page A = load_v(rv, so[i], voff); Page B = load_v(rv, so[i + 1], voff); SB()
#define QK2(A, B) xa = qk_page(A, qf); xb = qk_page(B, qf); SB()
#define SM(F, MF, i, fin) p = softmax_step<F, MF>(st, xa, xb, rem[i], rem[i + 1], g4, fin); SB()
#define PV(F, A, B) pv_step<F>(st, A, B, p); SB()
#define TRY_MERGE() do { if (__builtin_amdgcn_readfirstlane(ctl[kvh]) == 3u) { CB(); merge_record(st, rec, lane, g); merged = 1; } SB(); } while (0)
  f32x4 xa, xb; v4u p;
  LK2(K0, K1, 0); LV2(V0, V1, 0);
  TS2(2);
  if (rem[0] > 0) {
    QK2(K0, K1); LK2(K2, K3, 2); SM(true, false, 0, false); PV(true, V0, V1);
    TS2(3);
    LV2(V2, V3, 2);
    if (rem[2] > 0) {
      QK2(K2, K3); LK2(K4, K5, 4); SM(false, false, 2, false); PV(false, V2, V3);
      TS2(4);
      LV2(V4, V5, 4);
      if (rem[6] > 0) {
        QK2(K4, K5); LK2(K6, K7, 6); SM(false, false, 4, false); PV(false, V4, V5);
        LV2(V6, V7, 6);
        TRY_MERGE(); QK2(K6, K7); SM(false, true, 6, merged != 0); PV(false, V6, V7);
      } else if (rem[4] > 0) {
        TRY_MERGE(); QK2(K4, K5); SM(false, true, 4, merged != 0);
        TS2(5);
        PV(false, V4, V5);
      }
    }
  }
  asm volatile("" :: "v"(cpf));
  TS2(6);
  const unsigned e1_ = ep + 1u, hA = e1_ * 0x9E3779B1u, KX = (e1_ << 1) | 1u, hB = hA ^ KX;
  const __amdgpu_buffer_rsrc_t rw = __builtin_amdgcn_make_buffer_rsrc((void*)ws, 0, WS_BYTES, 0x00020000);
  unsigned t = 1;
  if (!merged) {
    t = 0;
    if (lane == 0) t = LDS_ADD(ctl + kvh, 1u);
    t = __builtin_amdgcn_readfirstlane(t);
    if (t != 0) {
      while (t != 3u && t != 4u) { __builtin_amdgcn_s_sleep(1); t = __builtin_amdgcn_readfirstlane(ctl[kvh]); }
      CB();
      merge_record(st, rec, lane, g);
      const float L = xg_sum(st.l);
      const f32x4 iv = rowb((L > 0.f) ? __builtin_amdgcn_rcpf(L) : 0.f);
      st.lse = (L > 0.f) ? fmaf(st.m, C2, __builtin_amdgcn_logf(L)) : -INFINITY;
#pragma unroll
      for (int m = 0; m < 8; ++m) st.acc[m] *= iv;
    }
  }
  if (t != 0) {
    const f32x4 lb = rowb(st.lse);
    const int vo = WS_VAL + ((((kvh * G + 4 * (g & 1)) * 4 + (n >> 2)) * NWG + c) * 8 + 2 * (n & 3) + (g >> 1)) * 16;
#pragma unroll
    for (int r = 0; r < 4; ++r) {
      const unsigned w0 = cvtpk(st.acc[0][r], st.acc[1][r]), w1 = cvtpk(st.acc[2][r], st.acc[3][r]);
      const unsigned w2 = cvtpk(st.acc[4][r], st.acc[5][r]), w3 = cvtpk(st.acc[6][r], st.acc[7][r]);
      const auto s0 = __builtin_amdgcn_permlane32_swap(w0, w2, false, false);
      const auto s1 = __builtin_amdgcn_permlane32_swap(w1, w3, false, false);
      const unsigned l = __float_as_uint(lb[r]);
      const v4u gr = {s0[0], l ^ hA, s1[0], l ^ hB};
      __builtin_amdgcn_raw_buffer_store_b128(gr, rw, (NO_EXCH ? OOB : vo) + r * (4 * NWG * 128), 0, AUX_SC1);
    }
    if (lane == 0) LDS_ADD(ctl + 4, 1u);
    TS(7);
  } else {
    const float lsum = xg_sum(st.l);
    if (lane < 32) {
#pragma unroll
      for (int m = 0; m < 8; ++m) *(f32x4*)(rec + (m * 32 + lane) * 4) = st.acc[m];
      if (lane < G) *(f32x2*)(rec + 1024 + 2 * lane) = f32x2{st.m, lsum};
    }
    LGKM0();
    if (lane == 0) LDS_ADD(ctl + kvh, 2u);
    TS2(8);
    if (NO_EXCH) { FLUSH_STAMPS(); return; }
    const __amdgpu_buffer_rsrc_t ro = __builtin_amdgcn_make_buffer_rsrc((void*)out, 0, H * D * 2, 0x00020000);
    const int h = c >> 3, j = c & 7;
    const int vo = WS_VAL + ((c >> 1) * NWG + 64 * kvh + (lane >> 2)) * 128 + (c & 1) * 64 + (lane & 3) * 16;
    const v4u rdesc = {(unsigned)(uintptr_t)ws, (unsigned)((uintptr_t)ws >> 32), (unsigned)WS_BYTES, 0x00020000u};
    v4u ra[4], rb[4];
#define KEEP_RING() asm volatile("" :: "{v[128:131]}"(ra[0]), "{v[132:135]}"(ra[1]), "{v[136:139]}"(ra[2]), "{v[140:143]}"(ra[3]), "{v[144:147]}"(rb[0]), "{v[148:151]}"(rb[1]), "{v[152:155]}"(rb[2]), "{v[156:159]}"(rb[3]))
    unsigned pass = 0;
    int dry = __builtin_amdgcn_readfirstlane(ctl[4]) == 0u;
#pragma clang loop unroll(disable)
    for (;;) {
      { int fv = dry; asm volatile("" : "+v"(fv)); dry = __builtin_amdgcn_readfirstlane(fv); }
      const int vod = dry ? OOB : vo;
      unsigned cnt = dry ? 1u : (1u << 20);
      unsigned gt[4], gu[4], gw[4];
      unsigned long long m_ma[4], m_mb[4], m_c[4], mx0, mx1;
      asm volatile(
          "s_nop 4\n\t"
          "s_mov_b64 %[mb0], -1\n\t"
          "s_mov_b64 %[mb1], -1\n\t"
          "s_mov_b64 %[mb2], -1\n\t"
          "s_mov_b64 %[mb3], -1\n\t"
          "buffer_load_dwordx4 v[128:131], %[vo], %[rs], 0 offen sc1\n\t"
          "buffer_load_dwordx4 v[132:135], %[vo], %[rs], 0 offen offset:2048 sc1\n\t"
          "buffer_load_dwordx4 v[136:139], %[vo], %[rs], %[s4k] offen sc1\n\t"
          "buffer_load_dwordx4 v[140:143], %[vo], %[rs], %[s4k] offen offset:2048 sc1\n\t"
          "s_sleep " STR(STAG) "\n\t"
          "buffer_load_dwordx4 v[144:147], %[vo], %[rs], 0 offen sc1\n\t"
          "buffer_load_dwordx4 v[148:151], %[vo], %[rs], 0 offen offset:2048 sc1\n\t"
          "buffer_load_dwordx4 v[152:155], %[vo], %[rs], %[s4k] offen sc1\n\t"
          "buffer_load_dwordx4 v[156:159], %[vo], %[rs], %[s4k] offen offset:2048 sc1\n\t"
          "1:\n\t"
          "s_waitcnt vmcnt(4)\n\t"
          "v_xor_b32 %[t0], v129, v131\n\t"
          "v_xor_b32 %[t1], v133, v135\n\t"
          "v_xor_b32 %[t2], v137, v139\n\t"
          "v_xor_b32 %[t3], v141, v143\n\t"
          "v_xor_b32_dpp %[u0], v129, v129 quad_perm:[1,0,3,2] row_mask:0xf bank_mask:0xf\n\t"
          "v_xor_b32_dpp %[u1], v133, v133 quad_perm:[1,0,3,2] row_mask:0xf bank_mask:0xf\n\t"
          "v_xor_b32_dpp %[u2], v137, v137 quad_perm:[1,0,3,2] row_mask:0xf bank_mask:0xf\n\t"
          "v_xor_b32_dpp %[u3], v141, v141 quad_perm:[1,0,3,2] row_mask:0xf bank_mask:0xf\n\t"
          "v_bitop3_b32 %[t0], %[t0], %[kx], %[u0] bitop3:0xbe\n\t"
          "v_bitop3_b32 %[t1], %[t1], %[kx], %[u1] bitop3:0xbe\n\t"
          "v_bitop3_b32 %[t2], %[t2], %[kx], %[u2] bitop3:0xbe\n\t"
          "v_bitop3_b32 %[t3], %[t3], %[kx], %[u3] bitop3:0xbe\n\t"
          "v_cmp_ne_u32 %[ma0], 0, %[t0]\n\t"
          "v_cmp_ne_u32 %[ma1], 0, %[t1]\n\t"
          "v_cmp_ne_u32 %[ma2], 0, %[t2]\n\t"
          "v_cmp_ne_u32 %[ma3], 0, %[t3]\n\t"
          "s_and_b64 %[c0], %[ma0], %[mb0]\n\t"
          "s_and_b64 %[c1], %[ma1], %[mb1]\n\t"
          "s_and_b64 %[c2], %[ma2], %[mb2]\n\t"
          "s_and_b64 %[c3], %[ma3], %[mb3]\n\t"
          "s_or_b64 %[x0], %[c0], %[c1]\n\t"
          "s_or_b64 %[x1], %[c2], %[c3]\n\t"
          "s_or_b64 %[x0], %[x0], %[x1]\n\t"
          "s_cbranch_scc0 2f\n\t"
          "s_mov_b64 exec, %[c0]\n\t"
          "buffer_load_dwordx4 v[128:131], %[vo], %[rs], 0 offen sc1\n\t"
          "s_mov_b64 exec, %[c1]\n\t"
          "buffer_load_dwordx4 v[132:135], %[vo], %[rs], 0 offen offset:2048 sc1\n\t"
          "s_mov_b64 exec, %[c2]\n\t"
          "buffer_load_dwordx4 v[136:139], %[vo], %[rs], %[s4k] offen sc1\n\t"
          "s_mov_b64 exec, %[c3]\n\t"
          "buffer_load_dwordx4 v[140:143], %[vo], %[rs], %[s4k] offen offset:2048 sc1\n\t"
          "s_mov_b64 exec, -1\n\t"
          "s_waitcnt vmcnt(4)\n\t"
          "v_xor_b32 %[t0], v145, v147\n\t"
          "v_xor_b32 %[t1], v149, v151\n\t"
          "v_xor_b32 %[t2], v153, v155\n\t"
          "v_xor_b32 %[t3], v157, v159\n\t"
          "v_xor_b32_dpp %[u0], v145, v145 quad_perm:[1,0,3,2] row_mask:0xf bank_mask:0xf\n\t"
          "v_xor_b32_dpp %[u1], v149, v149 quad_perm:[1,0,3,2] row_mask:0xf bank_mask:0xf\n\t"
          "v_xor_b32_dpp %[u2], v153, v153 quad_perm:[1,0,3,2] row_mask:0xf bank_mask:0xf\n\t"
          "v_xor_b32_dpp %[u3], v157, v157 quad_perm:[1,0,3,2] row_mask:0xf bank_mask:0xf\n\t"
          "v_bitop3_b32 %[t0], %[t0], %[kx], %[u0] bitop3:0xbe\n\t"
          "v_bitop3_b32 %[t1], %[t1], %[kx], %[u1] bitop3:0xbe\n\t"
          "v_bitop3_b32 %[t2], %[t2], %[kx], %[u2] bitop3:0xbe\n\t"
          "v_bitop3_b32 %[t3], %[t3], %[kx], %[u3] bitop3:0xbe\n\t"
          "v_cmp_ne_u32 %[mb0], 0, %[t0]\n\t"
          "v_cmp_ne_u32 %[mb1], 0, %[t1]\n\t"
          "v_cmp_ne_u32 %[mb2], 0, %[t2]\n\t"
          "v_cmp_ne_u32 %[mb3], 0, %[t3]\n\t"
          "s_and_b64 %[c0], %[ma0], %[mb0]\n\t"
          "s_and_b64 %[c1], %[ma1], %[mb1]\n\t"
          "s_and_b64 %[c2], %[ma2], %[mb2]\n\t"
          "s_and_b64 %[c3], %[ma3], %[mb3]\n\t"
          "s_or_b64 %[x0], %[c0], %[c1]\n\t"
          "s_or_b64 %[x1], %[c2], %[c3]\n\t"
          "s_or_b64 %[x0], %[x0], %[x1]\n\t"
          "s_cbranch_scc0 2f\n\t"
          "s_mov_b64 exec, %[c0]\n\t"
          "buffer_load_dwordx4 v[144:147], %[vo], %[rs], 0 offen sc1\n\t"
          "s_mov_b64 exec, %[c1]\n\t"
          "buffer_load_dwordx4 v[148:151], %[vo], %[rs], 0 offen offset:2048 sc1\n\t"
          "s_mov_b64 exec, %[c2]\n\t"
          "buffer_load_dwordx4 v[152:155], %[vo], %[rs], %[s4k] offen sc1\n\t"
          "s_mov_b64 exec, %[c3]\n\t"
          "buffer_load_dwordx4 v[156:159], %[vo], %[rs], %[s4k] offen offset:2048 sc1\n\t"
          "s_mov_b64 exec, -1\n\t"
          "s_sub_u32 %[cnt], %[cnt], 1\n\t"
          "s_cmp_lg_u32 %[cnt], 0\n\t"
          "s_cbranch_scc1 1b\n\t"
          "2:\n\t"
          "v_cndmask_b32 %[t0], v129, v145, %[ma0]\n\t"
          "v_cndmask_b32 %[u0], v128, v144, %[ma0]\n\t"
          "v_cndmask_b32 %[w0], v130, v146, %[ma0]\n\t"
          "v_cndmask_b32 %[t1], v133, v149, %[ma1]\n\t"
          "v_cndmask_b32 %[u1], v132, v148, %[ma1]\n\t"
          "v_cndmask_b32 %[w1], v134, v150, %[ma1]\n\t"
          "v_cndmask_b32 %[t2], v137, v153, %[ma2]\n\t"
          "v_cndmask_b32 %[u2], v136, v152, %[ma2]\n\t"
          "v_cndmask_b32 %[w2], v138, v154, %[ma2]\n\t"
          "v_cndmask_b32 %[t3], v141, v157, %[ma3]\n\t"
          "v_cndmask_b32 %[u3], v140, v156, %[ma3]\n\t"
          "v_cndmask_b32 %[w3], v142, v158, %[ma3]\n\t"
          "s_nop 1\n\t"
          : "={v[128:131]}"(ra[0]), "={v[132:135]}"(ra[1]), "={v[136:139]}"(ra[2]), "={v[140:143]}"(ra[3]), "={v[144:147]}"(rb[0]), "={v[148:151]}"(rb[1]), "={v[152:155]}"(rb[2]), "={v[156:159]}"(rb[3]), [t0] "=&v"(gt[0]), [t1] "=&v"(gt[1]), [t2] "=&v"(gt[2]), [t3] "=&v"(gt[3]), [u0] "=&v"(gu[0]), [u1] "=&v"(gu[1]), [u2] "=&v"(gu[2]), [u3] "=&v"(gu[3]), [w0] "=&v"(gw[0]), [w1] "=&v"(gw[1]), [w2] "=&v"(gw[2]), [w3] "=&v"(gw[3]), [ma0] "=&s"(m_ma[0]), [ma1] "=&s"(m_ma[1]), [ma2] "=&s"(m_ma[2]), [ma3] "=&s"(m_ma[3]), [mb0] "=&s"(m_mb[0]), [mb1] "=&s"(m_mb[1]), [mb2] "=&s"(m_mb[2]), [mb3] "=&s"(m_mb[3]), [c0] "=&s"(m_c[0]), [c1] "=&s"(m_c[1]), [c2] "=&s"(m_c[2]), [c3] "=&s"(m_c[3]), [x0] "=&s"(mx0), [x1] "=&s"(mx1), [cnt] "+s"(cnt)
          : [vo] "v"(vod), [rs] "s"(rdesc), [s4k] "s"(4096), [kx] "s"(KX)
          : "memory", "scc");
      if (!dry && cnt == 0u) { asm volatile("s_waitcnt vmcnt(0)" ::: "memory"); KEEP_RING(); return; }
      {
        float gl[4];
#pragma unroll
        for (int i = 0; i < 4; ++i) gl[i] = __uint_as_float(gt[i] ^ hA);
        float M = fmaxf(fmaxf(gl[0], gl[1]), fmaxf(gl[2], gl[3]));
        M = fmaxf(M, dpp<0x124>(M)); M = fmaxf(M, dpp<0x128>(M));
        M = xg_max(M);
        const float Ms = (M == -INFINITY) ? 0.f : M;
        float W = 0.f, a0 = 0.f, a1 = 0.f, a2 = 0.f, a3 = 0.f;
#pragma unroll
        for (int i = 0; i < 4; ++i) {
          const float e = __builtin_amdgcn_exp2f(gl[i] - Ms);
          W += e;
          a0 = fmaf(e, __uint_as_float(gu[i] << 16), a0); a1 = fmaf(e, __uint_as_float(gu[i] & 0xffff0000u), a1);
          a2 = fmaf(e, __uint_as_float(gw[i] << 16), a2); a3 = fmaf(e, __uint_as_float(gw[i] & 0xffff0000u), a3);
        }
        const auto s01 = __builtin_amdgcn_permlane32_swap(__float_as_uint(a0), __float_as_uint(a1), false, false);
        const auto s23 = __builtin_amdgcn_permlane32_swap(__float_as_uint(a2), __float_as_uint(a3), false, false);
        const float u01 = __uint_as_float(s01[0]) + __uint_as_float(s01[1]);
        const float u23 = __uint_as_float(s23[0]) + __uint_as_float(s23[1]);
        const auto sx = __builtin_amdgcn_permlane16_swap(__float_as_uint(u01), __float_as_uint(u23), false, false);
        float v = __uint_as_float(sx[0]) + __uint_as_float(sx[1]);
        v += dpp<0x124>(v); v += dpp<0x128>(v);
        W += dpp<0x124>(W); W += dpp<0x128>(W);
        W = xg_sum(W);
        float* slot = lds + LDS_FIN + (dry ? 4 : kvh) * FIN_ROW;
        unsigned tk = 0;
        if (n < 4) {
          slot[4 * n + 2 * (g & 1) + (g >> 1)] = v;
          if (lane == 0) {
            *(f32x2*)(slot + 32) = f32x2{M, W};
            CB();
            int ci = dry ? 6 : 5;
            asm volatile("" : "+v"(ci));
            tk = LDS_ADD(ctl + ci, 1u);
          }
        }
        CB();
        const float* fin = lds + LDS_FIN;
        float mw[4], ww[4], av[4];
#pragma unroll
        for (int i = 0; i < 4; ++i) {
          const f32x2 x = *(const f32x2*)(fin + i * FIN_ROW + 32);
          mw[i] = x[0]; ww[i] = x[1]; av[i] = fin[i * FIN_ROW + n];
        }
        CB();
        tk = __builtin_amdgcn_readfirstlane(tk);
        if (!dry && tk != 3u) {
          TS(11);
          FLUSH_STAMPS();
          KEEP_RING();
          return;
        }
        M = fmaxf(fmaxf(mw[0], mw[1]), fmaxf(mw[2], mw[3]));
        const float Mf = (M == -INFINITY) ? 0.f : M;
        float Wt = 0.f, at = 0.f;
#pragma unroll
        for (int i = 0; i < 4; ++i) {
          const float fi = __builtin_amdgcn_exp2f(mw[i] - Mf);
          Wt = fmaf(fi, ww[i], Wt); at = fmaf(fi, av[i], at);
        }
        if (!dry && ctx < LK && !(M > -1.0e9f * LOG2E + 2048.f)) {
          const float lm = -1.0e9f * LOG2E;
          const float Mall = fmaxf(M, lm);
          const float wv = (M == -INFINITY) ? 0.f : __builtin_amdgcn_exp2f(M - Mall);
          const float wm = __builtin_amdgcn_exp2f(lm - Mall);
          float s0 = 0.f;
          const int hk = h >> 3;
          for (int tk2 = ctx + g; tk2 < LK; tk2 += 4) {
            const unsigned short d = *(const unsigned short*)(vc + ((size_t)bt[tk2 / BS] * BS + (tk2 % BS)) * (HKV * D * 2) + hk * (D * 2) + (j * 16 + n) * 2);
            s0 += __uint_as_float((unsigned)d << 16);
          }
          s0 = xg_sum(s0);
          at = at * wv + s0 * wm;
          Wt = Wt * wv + wm * (float)(LK - ctx);
        }
        const float res = at * __builtin_amdgcn_rcpf(Wt);
        const unsigned pk = cvtpk(res, dpp<0xB1>(res));
        const int oo = (!dry && lane < 16 && !(lane & 1)) ? (h * D + j * 16 + lane) * 2 : OOB;
        __builtin_amdgcn_raw_buffer_store_b32(pk, ro, oo, 0, 0);
        if (!dry) {
          if (c == 0 && lane == 0) *(unsigned*)ws = e1_;
          KEEP_RING();
          pass = (1u << 20) - cnt;
          break;
        }
      }
      asm volatile("s_waitcnt vmcnt(0)" ::: "memory");
      KEEP_RING();
      dry = 0;
      TS2(9);
      unsigned spins = 0;
      while (__builtin_amdgcn_readfirstlane(ctl[4]) < POLL_START && ++spins < (1u << 22)) __builtin_amdgcn_s_sleep(2);
      TS(10);
    }
    TS(13);
  }
  FLUSH_STAMPS();
}

}

extern "C" void kernel_launch(void* const* d_in, const int* in_sizes, int n_in, void* d_out, int out_size, void* d_ws,
                              size_t ws_size, hipStream_t stream) {
  attn_decode<<<NWG, NTHR, 0, stream>>>((const char*)d_in[0], (const char*)d_in[1], (const char*)d_in[2], (const int*)d_in[3],
                                        (const int*)d_in[4], (char*)d_ws, (char*)d_out, in_sizes[1] * 2, 0);
}
```

```cpp
#include <hip/hip_runtime.h>
#include <hip/hip_bf16.h>
#include <stdint.h>

#define POLL_START 4u
#define DELAY0 0
#define DELAY1 0
#define DRY_OOB 1
#define NO_EXCH 0
#define STR_(x) #x
#define STR(x) STR_(x)
#define STAG 2
#define SCHED 2
#define ROT 1
#define CODE_PF 7424
#define EARLY_SIG 0
#define PMODE 0
#define PRIO 1
#define SWAP 0
#define EVENX 1
#define KV_AUX 2

#define TS(i)
#define TS2(i)
#define FLUSH_STAMPS()

namespace {

constexpr int H = 32, HKV = 4, D = 128, BS = 16, NB = 4096, G = H / HKV;
constexpr int LK = NB * BS;
constexpr int NWG = 256, NWAVE = 8, NTHR = NWAVE * 64;
constexpr int UPW = NB / (NWG * 2);
static_assert(UPW == 8, "straight-line body is written for 8 pages per wave");
constexpr float SCALE = 0.08838834764831845f;
constexpr float LOG2E = 1.4426950408889634f;
constexpr float C2 = SCALE * LOG2E;
constexpr int PSTEP = 2 * NWG * BS;
constexpr int OOB = 0x40000000;
constexpr int WS_VAL = 4096;
constexpr int WS_BYTES = WS_VAL + H * 4 * NWG * 128;
constexpr int AUX_SC1 = 16;

typedef unsigned v4u __attribute__((ext_vector_type(4)));
typedef unsigned v2u __attribute__((ext_vector_type(2)));
typedef float f32x4 __attribute__((ext_vector_type(4)));
typedef __bf16 bf16x8 __attribute__((ext_vector_type(8)));
typedef __bf16 bf16x2 __attribute__((ext_vector_type(2)));
typedef float f32x2 __attribute__((ext_vector_type(2)));
#define LAS __attribute__((address_space(3)))
#define CAS __attribute__((address_space(4)))

__device__ __forceinline__ f32x4 mfma(v4u a, v4u b, f32x4 c) {
  return __builtin_amdgcn_mfma_f32_16x16x32_bf16(__builtin_bit_cast(bf16x8, a), __builtin_bit_cast(bf16x8, b), c, 0, 0, 0);
}
__device__ __forceinline__ unsigned cvtpk(float lo, float hi) {
  f32x2 v = {lo, hi};
  bf16x2 r = __builtin_convertvector(v, bf16x2);
  return __builtin_bit_cast(unsigned, r);
}
template <int CTRL> __device__ __forceinline__ float dpp(float x) {
  return __builtin_bit_cast(float, __builtin_amdgcn_mov_dpp(__builtin_bit_cast(int, x), CTRL, 0xf, 0xf, true));
}
template <int CTRL> __device__ __forceinline__ unsigned dppu(unsigned x) {
  return (unsigned)__builtin_amdgcn_mov_dpp((int)x, CTRL, 0xf, 0xf, true);
}
__device__ __forceinline__ float xg_max(float x) {
  auto s = __builtin_amdgcn_permlane16_swap(__float_as_uint(x), __float_as_uint(x), false, false);
  x = fmaxf(__uint_as_float(s[0]), __uint_as_float(s[1]));
  auto t = __builtin_amdgcn_permlane32_swap(__float_as_uint(x), __float_as_uint(x), false, false);
  return fmaxf(__uint_as_float(t[0]), __uint_as_float(t[1]));
}
__device__ __forceinline__ float xg_sum(float x) {
  auto s = __builtin_amdgcn_permlane16_swap(__float_as_uint(x), __float_as_uint(x), false, false);
  x = __uint_as_float(s[0]) + __uint_as_float(s[1]);
  auto t = __builtin_amdgcn_permlane32_swap(__float_as_uint(x), __float_as_uint(x), false, false);
  return __uint_as_float(t[0]) + __uint_as_float(t[1]);
}
template <int R> __device__ __forceinline__ float rowb1(float x) {
  int t = __builtin_amdgcn_update_dpp(0, __builtin_bit_cast(int, x), 0x150 + R, 0x5, 0xf, false);
  t = __builtin_amdgcn_update_dpp(t, __builtin_bit_cast(int, x), 0x150 + 4 + R, 0xa, 0xf, false);
  return __builtin_bit_cast(float, t);
}
__device__ __forceinline__ f32x4 rowb(float x) { return f32x4{rowb1<0>(x), rowb1<1>(x), rowb1<2>(x), rowb1<3>(x)}; }

struct Page { v4u r[4]; };

__device__ __forceinline__ Page load_k(__amdgpu_buffer_rsrc_t rs, int so, int vo) {
  Page p;
  p.r[0] = __builtin_amdgcn_raw_buffer_load_b128(rs, vo, so, KV_AUX);
  p.r[1] = __builtin_amdgcn_raw_buffer_load_b128(rs, vo + 128, so, KV_AUX);
  p.r[2] = __builtin_amdgcn_raw_buffer_load_b128(rs, vo, so + 8192, KV_AUX);
  p.r[3] = __builtin_amdgcn_raw_buffer_load_b128(rs, vo + 128, so + 8192, KV_AUX);
  return p;
}
__device__ __forceinline__ Page load_v(__amdgpu_buffer_rsrc_t rs, int so, int vo) {
  Page p;
  p.r[0] = __builtin_amdgcn_raw_buffer_load_b128(rs, vo, so, KV_AUX);
  p.r[1] = __builtin_amdgcn_raw_buffer_load_b128(rs, vo + 1024, so, KV_AUX);
  p.r[2] = __builtin_amdgcn_raw_buffer_load_b128(rs, vo + 2048, so, KV_AUX);
  p.r[3] = __builtin_amdgcn_raw_buffer_load_b128(rs, vo + 3072, so, KV_AUX);
  return p;
}

struct QFrag { v4u q[2]; };
__device__ __forceinline__ f32x4 qk_page(const Page& k, const QFrag& q) {
  const f32x4 z = {0.f, 0.f, 0.f, 0.f};
  f32x4 d0 = mfma(k.r[0], q.q[0], z);
  f32x4 d1 = mfma(k.r[2], q.q[0], z);
  d0 = mfma(k.r[1], q.q[1], d0);
  d1 = mfma(k.r[3], q.q[1], d1);
  f32x4 x;
#pragma unroll
  for (int r = 0; r < 4; ++r) {
    auto s = __builtin_amdgcn_permlane32_swap(__float_as_uint(d0[r]), __float_as_uint(d1[r]), false, false);
    x[r] = __uint_as_float(s[0]) + dpp<0x128>(__uint_as_float(s[1]));
  }
  return x;
}

struct State { float m, l, lse; f32x4 acc[8]; };

template <bool LAST>
__device__ __forceinline__ v4u softmax_step(State& st, f32x4 xa, f32x4 xb, int rema, int g4, bool fin) {
  if (LAST && rema - PSTEP < BS) {
    const int lima = rema - g4, limb = lima - PSTEP;
#pragma unroll
    for (int r = 0; r < 4; ++r) {
      xa[r] = (r < lima) ? xa[r] : -INFINITY;
      xb[r] = (r < limb) ? xb[r] : -INFINITY;
    }
  }
  float mx = fmaxf(fmaxf(fmaxf(xa[0], xa[1]), fmaxf(xa[2], xa[3])), fmaxf(fmaxf(xb[0], xb[1]), fmaxf(xb[2], xb[3])));
  mx = xg_max(mx);
  const float mn = fmaxf(st.m, mx);
  const float mc = (LAST && mn == -INFINITY) ? 0.f : mn * C2;
  float pa[4], pb[4];
#pragma unroll
  for (int r = 0; r < 4; ++r) {
    pa[r] = __builtin_amdgcn_exp2f(fmaf(xa[r], C2, -mc));
    pb[r] = __builtin_amdgcn_exp2f(fmaf(xb[r], C2, -mc));
  }
  const float ps = ((pa[0] + pa[1]) + (pa[2] + pa[3])) + ((pb[0] + pb[1]) + (pb[2] + pb[3]));
  float alpha = __builtin_amdgcn_exp2f(fmaf(st.m, C2, -mc));
  st.l = fmaf(st.l, alpha, ps);
  if (LAST) {
    st.l = xg_sum(st.l);
    const float inv = fin ? (st.l > 0.f ? __builtin_amdgcn_rcpf(st.l) : 0.f) : 1.f;
    st.lse = mc + __builtin_amdgcn_logf(st.l);
    alpha *= inv;
#pragma unroll
    for (int r = 0; r < 4; ++r) { pa[r] *= inv; pb[r] *= inv; }
  }
  const f32x4 av = rowb(alpha);
#pragma unroll
  for (int m = 0; m < 8; ++m) st.acc[m] *= av;
  st.m = mn;
  v4u p;
  p[0] = cvtpk(pa[0], pa[1]);
  p[1] = cvtpk(pa[2], pa[3]);
  p[2] = cvtpk(pb[0], pb[1]);
  p[3] = cvtpk(pb[2], pb[3]);
  return p;
}

__device__ __forceinline__ void pv_step(State& st, const Page& va, const Page& vb, v4u p) {
#pragma unroll
  for (int m = 0; m < 8; ++m) {
    const int w = m >> 1;
    const unsigned sel = (m & 1) ? 0x07060302u : 0x05040100u;
    v4u f;
    f[0] = __builtin_amdgcn_perm(va.r[1][w], va.r[0][w], sel);
    f[1] = __builtin_amdgcn_perm(va.r[3][w], va.r[2][w], sel);
    f[2] = __builtin_amdgcn_perm(vb.r[1][w], vb.r[0][w], sel);
    f[3] = __builtin_amdgcn_perm(vb.r[3][w], vb.r[2][w], sel);
    st.acc[m] = mfma(p, f, st.acc[m]);
  }
}

constexpr int REC = 8 * 32 * 4 + 16;
constexpr int LDS_CTL = HKV * REC;
constexpr int LDS_FIN = LDS_CTL + 8;
constexpr int FIN_ROW = 36;
constexpr int LDS_WORDS = LDS_FIN + 8 * FIN_ROW;

__device__ __forceinline__ void merge_record(State& st, const float* rec, int lane, int g) {
  f32x4 o[8];
#pragma unroll
  for (int m = 0; m < 8; ++m) o[m] = *(const f32x4*)(rec + (m * 32 + (lane & 31)) * 4);
  const f32x2 ml = *(const f32x2*)(rec + 1024 + 2 * (lane & 7));
  const float M = fmaxf(st.m, ml[0]);
  const float Mc = (M == -INFINITY) ? 0.f : M * C2;
  const float e1 = __builtin_amdgcn_exp2f(fmaf(st.m, C2, -Mc)), e2 = __builtin_amdgcn_exp2f(fmaf(ml[0], C2, -Mc));
  st.l = fmaf(st.l, e1, g == 0 ? e2 * ml[1] : 0.f);
  st.m = M;
  const f32x4 v1 = rowb(e1), v2 = rowb(e2);
#pragma unroll
  for (int m = 0; m < 8; ++m) st.acc[m] = st.acc[m] * v1 + o[m] * v2;
}

#define SB() do { asm volatile("" ::: "memory"); __builtin_amdgcn_sched_barrier(0); } while (0)
#define CB() asm volatile("" ::: "memory")
#define LGKM0() asm volatile("s_waitcnt lgkmcnt(0)" ::: "memory")
#define LDS_ADD(p, v) __hip_atomic_fetch_add((LAS unsigned*)(p), (v), __ATOMIC_RELAXED, __HIP_MEMORY_SCOPE_WORKGROUP)

__global__ void __launch_bounds__(NTHR, 2)
attn_decode(const char* q, const char* kc, const char* vc, const int* bt, const int* ctx_lens,
            char* ws, char* out, int kv_bytes, int pad_) {
  __shared__ __attribute__((aligned(16))) float lds[LDS_WORDS];
  const int tid = threadIdx.x, lane = tid & 63;
  const int w = __builtin_amdgcn_readfirstlane(tid >> 6);
  unsigned cpf = 0;
  {
    const __amdgpu_buffer_rsrc_t rc = __builtin_amdgcn_make_buffer_rsrc((void*)(__builtin_amdgcn_s_getpc() & ~127ull), 0, CODE_PF, 0x00020000);
    cpf = __builtin_amdgcn_raw_buffer_load_b32(rc, tid * 128, 0, 0);
    asm volatile("" ::: "memory");
    __builtin_amdgcn_sched_barrier(0);
  }
  TS(0);
  const int kvh = w & 3, sub = w >> 2, c = blockIdx.x;
  const int n = lane & 15, g = lane >> 4;
  asm volatile("" :: "s"(q), "s"(kc), "s"(vc), "s"(bt), "s"(ctx_lens), "s"(ws), "s"(out), "s"(kv_bytes));

  const int par = SWAP ? 1 - sub : sub;
  const int pc = EVENX == 1 ? (((c & 1) << 7) | (((c >> 1) + 32 * kvh) & 127))
               : EVENX == 2 ? ((((c & 1) ^ 1) << 7) | (((c >> 1) + 32 * kvh) & 127)) : ((c + 64 * kvh) & (NWG - 1));
  const __amdgpu_buffer_rsrc_t rb = __builtin_amdgcn_make_buffer_rsrc((void*)bt, 0, NB * 4, 0x00020000);
  const int physv = __builtin_amdgcn_raw_buffer_load_b32(rb, lane * (2 * NWG * 4), (par * NWG + pc) * 4, 0);
  int ctx = *(const CAS int*)ctx_lens;
  const unsigned ep = *(const CAS unsigned*)ws;
  volatile LAS unsigned* ctl = (volatile LAS unsigned*)(lds + LDS_CTL);
  if (tid < 8) ctl[tid] = 0u;
  if (sub == 0) __builtin_amdgcn_s_setprio(1);

  QFrag qf;
  const char* qrow = q + (kvh * G + (n & 7)) * (D * 2) + (n >> 3) * 64 + g * 16;
  qf.q[0] = *(const v4u*)(qrow);
  qf.q[1] = *(const v4u*)(qrow + 128);
  const int koff = (n & 7) * 1024 + (n >> 3) * 64 + g * 16;
  const int voff = g * 4096 + n * 16;
  const __amdgpu_buffer_rsrc_t rk = __builtin_amdgcn_make_buffer_rsrc((void*)(kc + kvh * 256), 0, kv_bytes - kvh * 256, 0x00020000);
  const __amdgpu_buffer_rsrc_t rv = __builtin_amdgcn_make_buffer_rsrc((void*)(vc + kvh * 256), 0, kv_bytes - kvh * 256, 0x00020000);
  const int g4 = 4 * g;
  State st;
  st.m = -INFINITY; st.l = 0.f; st.lse = -INFINITY;
#pragma unroll
  for (int m = 0; m < 8; ++m) st.acc[m] = f32x4{0.f, 0.f, 0.f, 0.f};
  float* rec = lds + kvh * REC;

  asm volatile("" :: "s"(ctx), "s"(ep));
  LGKM0();
  __builtin_amdgcn_s_barrier();
  SB();
  ctx = ctx < 0 ? 0 : (ctx > LK ? LK : ctx);
  int rem = ctx - (par * NWG + pc) * BS;
  const int sov = (rem - lane * PSTEP > 0) ? (physv & 0xffff) << 14 : OOB;

  TS(1);
  int s0 = __builtin_amdgcn_readlane(sov, 0), s1 = __builtin_amdgcn_readlane(sov, 1);
  Page KA = load_k(rk, s0, koff), KB = load_k(rk, s1, koff);
  Page VA = load_v(rv, s0, voff), VB = load_v(rv, s1, voff);
  SB();
  TS2(2);
#pragma clang loop unroll(disable)
  for (int i = 2; rem - 2 * PSTEP > 0; i += 2, rem -= 2 * PSTEP) {
    s0 = __builtin_amdgcn_readlane(sov, i); s1 = __builtin_amdgcn_readlane(sov, i + 1);
    const f32x4 xa = qk_page(KA, qf), xb = qk_page(KB, qf);
    SB();
    KA = load_k(rk, s0, koff); KB = load_k(rk, s1, koff);
    SB();
    const v4u p = softmax_step<false>(st, xa, xb, 0, 0, false);
    SB();
    pv_step(st, VA, VB, p);
    SB();
    VA = load_v(rv, s0, voff); VB = load_v(rv, s1, voff);
    SB();
  }
  TS2(4);
  if (sub) {
    while (__builtin_amdgcn_readfirstlane(ctl[kvh]) == 0u) __builtin_amdgcn_s_sleep(1);
    CB();
    merge_record(st, rec, lane, g);
  }
  SB();
  const unsigned e1_ = ep + 1u, hA = e1_ * 0x9E3779B1u, KX = (e1_ << 1) | 1u, hB = hA ^ KX;
  const __amdgpu_buffer_rsrc_t rw = __builtin_amdgcn_make_buffer_rsrc((void*)ws, 0, WS_BYTES, 0x00020000);
  f32x4 lb; v4u ta, tb;
  int pvo = WS_VAL + ((((kvh * G + 4 * (g & 1)) * 4 + (n >> 2)) * NWG + c) * 8 + 2 * (n & 3) + (g >> 1)) * 16;
  asm volatile("" : "+v"(pvo));
  {
    const f32x4 xa = qk_page(KA, qf), xb = qk_page(KB, qf);
    const v4u p = softmax_step<true>(st, xa, xb, rem, g4, sub != 0);
    lb = rowb(st.lse);
#pragma unroll
    for (int r = 0; r < 4; ++r) { ta[r] = __float_as_uint(lb[r]) ^ hA; tb[r] = __float_as_uint(lb[r]) ^ hB; }
    asm volatile("" : "+v"(ta), "+v"(tb));
    SB();
    TS2(5);
    pv_step(st, VA, VB, p);
  }
  asm volatile("" :: "v"(cpf));
  TS2(6);
  if (sub) {
    const int vo = pvo;
#pragma unroll
    for (int r = 0; r < 4; ++r) {
      const unsigned w0 = cvtpk(st.acc[0][r], st.acc[1][r]), w1 = cvtpk(st.acc[2][r], st.acc[3][r]);
      const unsigned w2 = cvtpk(st.acc[4][r], st.acc[5][r]), w3 = cvtpk(st.acc[6][r], st.acc[7][r]);
      const auto s0 = __builtin_amdgcn_permlane32_swap(w0, w2, false, false);
      const auto s1 = __builtin_amdgcn_permlane32_swap(w1, w3, false, false);
      const v4u gr = {s0[0], ta[r], s1[0], tb[r]};
      __builtin_amdgcn_raw_buffer_store_b128(gr, rw, (NO_EXCH ? OOB : vo) + r * (4 * NWG * 128), 0, AUX_SC1);
    }
    if (lane == 0) LDS_ADD(ctl + 4, 1u);
    TS(7);
  } else {
    if (lane < 32) {
#pragma unroll
      for (int m = 0; m < 8; ++m) *(f32x4*)(rec + (m * 32 + lane) * 4) = st.acc[m];
      if (lane < G) *(f32x2*)(rec + 1024 + 2 * lane) = f32x2{st.m, st.l};
    }
    CB();
    if (lane == 0) ctl[kvh] = 1u;
    __builtin_amdgcn_s_setprio(0);
    TS2(8);
  }
  if (NO_EXCH || (c & 1)) { FLUSH_STAMPS(); return; }
  {
    const __amdgpu_buffer_rsrc_t ro = __builtin_amdgcn_make_buffer_rsrc((void*)out, 0, H * D * 2, 0x00020000);
    const int h = c >> 3, u = (c >> 1) & 3;
    const int vo = WS_VAL + ((c >> 1) * NWG + 32 * w) * 128 + lane * 16;
    const v4u rdesc = {(unsigned)(uintptr_t)ws, (unsigned)((uintptr_t)ws >> 32), (unsigned)WS_BYTES, 0x00020000u};
    v4u ra[4], rb[4];
#define KEEP_RING() asm volatile("" :: "{v[128:131]}"(ra[0]), "{v[132:135]}"(ra[1]), "{v[136:139]}"(ra[2]), "{v[140:143]}"(ra[3]), "{v[144:147]}"(rb[0]), "{v[148:151]}"(rb[1]), "{v[152:155]}"(rb[2]), "{v[156:159]}"(rb[3]))
    const int oo = (lane < 32 && !(lane & 1)) ? (h * D + u * 32 + lane) * 2 : OOB;
    unsigned pass = 0;
    { unsigned sp0 = 0; while (__builtin_amdgcn_readfirstlane(ctl[4]) < POLL_START && ++sp0 < (1u << 22)) __builtin_amdgcn_s_sleep(1); }
    TS(10);
    unsigned cnt = 1u << 20;
    unsigned gt[4], gu[4], gw[4];
    unsigned long long m_ma[4], m_mb[4], m_c[4], mx0, mx1;
    asm volatile(
        "s_nop 4\n\t"
        "buffer_load_dwordx4 v[128:131], %[vo], %[rs], 0 offen sc1\n\t"
        "buffer_load_dwordx4 v[132:135], %[vo], %[rs], 0 offen offset:1024 sc1\n\t"
        "buffer_load_dwordx4 v[136:139], %[vo], %[rs], 0 offen offset:2048 sc1\n\t"
        "buffer_load_dwordx4 v[140:143], %[vo], %[rs], 0 offen offset:3072 sc1\n\t"
        "s_mov_b64 %[mb0], -1\n\t"
        "v_mov_b32 v145, 0\n\t"
        "v_mov_b32 v147, 0\n\t"
        "s_mov_b64 %[mb1], -1\n\t"
        "v_mov_b32 v149, 0\n\t"
        "v_mov_b32 v151, 0\n\t"
        "s_mov_b64 %[mb2], -1\n\t"
        "v_mov_b32 v153, 0\n\t"
        "v_mov_b32 v155, 0\n\t"
        "s_mov_b64 %[mb3], -1\n\t"
        "v_mov_b32 v157, 0\n\t"
        "v_mov_b32 v159, 0\n\t"
        "1:\n\t"
        "s_waitcnt vmcnt(0)\n\t"
        "v_xor_b32 %[t0], v129, v131\n\t"
        "v_xor_b32 %[t1], v133, v135\n\t"
        "v_xor_b32 %[t2], v137, v139\n\t"
        "v_xor_b32 %[t3], v141, v143\n\t"
        "v_xor_b32_dpp %[u0], v129, v129 quad_perm:[1,0,3,2] row_mask:0xf bank_mask:0xf\n\t"
        "v_xor_b32_dpp %[u1], v133, v133 quad_perm:[1,0,3,2] row_mask:0xf bank_mask:0xf\n\t"
        "v_xor_b32_dpp %[u2], v137, v137 quad_perm:[1,0,3,2] row_mask:0xf bank_mask:0xf\n\t"
        "v_xor_b32_dpp %[u3], v141, v141 quad_perm:[1,0,3,2] row_mask:0xf bank_mask:0xf\n\t"
        "v_bitop3_b32 %[t0], %[t0], %[kx], %[u0] bitop3:0xbe\n\t"
        "v_bitop3_b32 %[t1], %[t1], %[kx], %[u1] bitop3:0xbe\n\t"
        "v_bitop3_b32 %[t2], %[t2], %[kx], %[u2] bitop3:0xbe\n\t"
        "v_bitop3_b32 %[t3], %[t3], %[kx], %[u3] bitop3:0xbe\n\t"
        "v_cmp_ne_u32 %[ma0], 0, %[t0]\n\t"
        "v_cmp_ne_u32 %[ma1], 0, %[t1]\n\t"
        "v_cmp_ne_u32 %[ma2], 0, %[t2]\n\t"
        "v_cmp_ne_u32 %[ma3], 0, %[t3]\n\t"
        "s_and_b64 %[c0], %[ma0], %[mb0]\n\t"
        "s_and_b64 %[c1], %[ma1], %[mb1]\n\t"
        "s_and_b64 %[c2], %[ma2], %[mb2]\n\t"
        "s_and_b64 %[c3], %[ma3], %[mb3]\n\t"
        "s_or_b64 %[x0], %[c0], %[c1]\n\t"
        "s_or_b64 %[x1], %[c2], %[c3]\n\t"
        "s_or_b64 %[x0], %[x0], %[x1]\n\t"
        "s_cbranch_scc0 2f\n\t"
        "s_sleep " STR(STAG) "\n\t"
        "s_mov_b64 exec, %[c0]\n\t"
        "buffer_load_dwordx4 v[128:131], %[vo], %[rs], 0 offen sc1\n\t"
        "s_mov_b64 exec, %[c1]\n\t"
        "buffer_load_dwordx4 v[132:135], %[vo], %[rs], 0 offen offset:1024 sc1\n\t"
        "s_mov_b64 exec, %[c2]\n\t"
        "buffer_load_dwordx4 v[136:139], %[vo], %[rs], 0 offen offset:2048 sc1\n\t"
        "s_mov_b64 exec, %[c3]\n\t"
        "buffer_load_dwordx4 v[140:143], %[vo], %[rs], 0 offen offset:3072 sc1\n\t"
        "s_mov_b64 exec, -1\n\t"
        "s_sub_u32 %[cnt], %[cnt], 1\n\t"
        "s_cmp_lg_u32 %[cnt], 0\n\t"
        "s_cbranch_scc1 1b\n\t"
        "2:\n\t"
        "v_cndmask_b32 %[t0], v129, v145, %[ma0]\n\t"
        "v_cndmask_b32 %[u0], v128, v144, %[ma0]\n\t"
        "v_cndmask_b32 %[w0], v130, v146, %[ma0]\n\t"
        "v_cndmask_b32 %[t1], v133, v149, %[ma1]\n\t"
        "v_cndmask_b32 %[u1], v132, v148, %[ma1]\n\t"
        "v_cndmask_b32 %[w1], v134, v150, %[ma1]\n\t"
        "v_cndmask_b32 %[t2], v137, v153, %[ma2]\n\t"
        "v_cndmask_b32 %[u2], v136, v152, %[ma2]\n\t"
        "v_cndmask_b32 %[w2], v138, v154, %[ma2]\n\t"
        "v_cndmask_b32 %[t3], v141, v157, %[ma3]\n\t"
        "v_cndmask_b32 %[u3], v140, v156, %[ma3]\n\t"
        "v_cndmask_b32 %[w3], v142, v158, %[ma3]\n\t"
        "s_nop 1\n\t"
        : "={v[128:131]}"(ra[0]), "={v[132:135]}"(ra[1]), "={v[136:139]}"(ra[2]), "={v[140:143]}"(ra[3]), "={v[144:147]}"(rb[0]), "={v[148:151]}"(rb[1]), "={v[152:155]}"(rb[2]), "={v[156:159]}"(rb[3]), [t0] "=&v"(gt[0]), [t1] "=&v"(gt[1]), [t2] "=&v"(gt[2]), [t3] "=&v"(gt[3]), [u0] "=&v"(gu[0]), [u1] "=&v"(gu[1]), [u2] "=&v"(gu[2]), [u3] "=&v"(gu[3]), [w0] "=&v"(gw[0]), [w1] "=&v"(gw[1]), [w2] "=&v"(gw[2]), [w3] "=&v"(gw[3]), [ma0] "=&s"(m_ma[0]), [ma1] "=&s"(m_ma[1]), [ma2] "=&s"(m_ma[2]), [ma3] "=&s"(m_ma[3]), [mb0] "=&s"(m_mb[0]), [mb1] "=&s"(m_mb[1]), [mb2] "=&s"(m_mb[2]), [mb3] "=&s"(m_mb[3]), [c0] "=&s"(m_c[0]), [c1] "=&s"(m_c[1]), [c2] "=&s"(m_c[2]), [c3] "=&s"(m_c[3]), [x0] "=&s"(mx0), [x1] "=&s"(mx1), [cnt] "+s"(cnt)
        : [vo] "v"(vo), [rs] "s"(rdesc), [s4k] "s"(4096), [kx] "s"(KX)
        : "memory", "scc");
    if (cnt == 0u) { asm volatile("s_waitcnt vmcnt(0)" ::: "memory"); KEEP_RING(); return; }
    {
      float gl[4];
#pragma unroll
      for (int i = 0; i < 4; ++i) gl[i] = __uint_as_float(gt[i] ^ hA);
      float M = fmaxf(fmaxf(gl[0], gl[1]), fmaxf(gl[2], gl[3]));
      M = fmaxf(M, dpp<0x128>(M));
      M = xg_max(M);
      const float Ms = (M == -INFINITY) ? 0.f : M;
      float W = 0.f, a0 = 0.f, a1 = 0.f, a2 = 0.f, a3 = 0.f;
#pragma unroll
      for (int i = 0; i < 4; ++i) {
        const float e = __builtin_amdgcn_exp2f(gl[i] - Ms);
        W += e;
        a0 = fmaf(e, __uint_as_float(gu[i] << 16), a0); a1 = fmaf(e, __uint_as_float(gu[i] & 0xffff0000u), a1);
        a2 = fmaf(e, __uint_as_float(gw[i] << 16), a2); a3 = fmaf(e, __uint_as_float(gw[i] & 0xffff0000u), a3);
      }
      const auto s01 = __builtin_amdgcn_permlane32_swap(__float_as_uint(a0), __float_as_uint(a1), false, false);
      const auto s23 = __builtin_amdgcn_permlane32_swap(__float_as_uint(a2), __float_as_uint(a3), false, false);
      const float u01 = __uint_as_float(s01[0]) + __uint_as_float(s01[1]);
      const float u23 = __uint_as_float(s23[0]) + __uint_as_float(s23[1]);
      const auto sx = __builtin_amdgcn_permlane16_swap(__float_as_uint(u01), __float_as_uint(u23), false, false);
      float v = __uint_as_float(sx[0]) + __uint_as_float(sx[1]);
      v += dpp<0x128>(v);
      W += dpp<0x128>(W);
      W = xg_sum(W);
      float* slot = lds + LDS_FIN + w * FIN_ROW;
      unsigned tk = 0;
      if (n < 8) {
        slot[4 * n + 2 * (g & 1) + (g >> 1)] = v;
        if (lane == 0) {
          *(f32x2*)(slot + 32) = f32x2{M, W};
          CB();
          int ci = 5;
          asm volatile("" : "+v"(ci));
          tk = LDS_ADD(ctl + ci, 1u);
        }
      }
      CB();
      const float* fin = lds + LDS_FIN;
      const int dd = lane & 31;
      float mw[8], ww[8], av[8];
#pragma unroll
      for (int i = 0; i < 8; ++i) {
        const f32x2 x = *(const f32x2*)(fin + i * FIN_ROW + 32);
        mw[i] = x[0]; ww[i] = x[1]; av[i] = fin[i * FIN_ROW + dd];
      }
      CB();
      tk = __builtin_amdgcn_readfirstlane(tk);
      if (tk != 7u) {
        TS(11);
        FLUSH_STAMPS();
        KEEP_RING();
        return;
      }
      M = fmaxf(fmaxf(fmaxf(mw[0], mw[1]), fmaxf(mw[2], mw[3])), fmaxf(fmaxf(mw[4], mw[5]), fmaxf(mw[6], mw[7])));
      const float Mf = (M == -INFINITY) ? 0.f : M;
      float Wt = 0.f, at = 0.f;
#pragma unroll
      for (int i = 0; i < 8; ++i) {
        const float fi = __builtin_amdgcn_exp2f(mw[i] - Mf);
        Wt = fmaf(fi, ww[i], Wt); at = fmaf(fi, av[i], at);
      }
      if (__builtin_expect(ctx < LK && !(M > -1.0e9f * LOG2E + 2048.f), 0)) {
        const float lm = -1.0e9f * LOG2E;
        const float Mall = fmaxf(M, lm);
        const float wv = (M == -INFINITY) ? 0.f : __builtin_amdgcn_exp2f(M - Mall);
        const float wm = __builtin_amdgcn_exp2f(lm - Mall);
        float s0 = 0.f;
        const int hk = h >> 3;
        for (int tk2 = ctx + (lane >> 5); tk2 < LK; tk2 += 2) {
          const unsigned short d = *(const unsigned short*)(vc + ((size_t)bt[tk2 / BS] * BS + (tk2 % BS)) * (HKV * D * 2) + hk * (D * 2) + (u * 32 + dd) * 2);
          s0 += __uint_as_float((unsigned)d << 16);
        }
        s0 += __shfl_xor(s0, 32);
        at = at * wv + s0 * wm;
        Wt = Wt * wv + wm * (float)(LK - ctx);
      }
      const float res = at * __builtin_amdgcn_rcpf(Wt);
      const unsigned pk = cvtpk(res, dpp<0xB1>(res));
      __builtin_amdgcn_raw_buffer_store_b32(pk, ro, oo, 0, 0);
      if (c == 0 && lane == 0) __hip_atomic_store((unsigned*)ws, e1_, __ATOMIC_RELAXED, __HIP_MEMORY_SCOPE_AGENT);
      KEEP_RING();
      pass = (1u << 20) - cnt;
    }
    TS(13);
  }
  FLUSH_STAMPS();
}

}

extern "C" void kernel_launch(void* const* d_in, const int* in_sizes, int n_in, void* d_out, int out_size, void* d_ws,
                              size_t ws_size, hipStream_t stream) {
  attn_decode<<<NWG, NTHR, 0, stream>>>((const char*)d_in[0], (const char*)d_in[1], (const char*)d_in[2], (const int*)d_in[3],
                                        (const int*)d_in[4], (char*)d_ws, (char*)d_out, in_sizes[1] * 2, 0);
}
```

```cpp
#include <hip/hip_runtime.h>
#include <hip/hip_bf16.h>
#include <stdint.h>

namespace {

constexpr int H = 32, HKV = 4, D = 128, BS = 16, NB = 4096, G = H / HKV;
constexpr int LK = NB * BS;
constexpr int NWG = 256, NWAVE = 8, NTHR = NWAVE * 64;
constexpr float SCALE = 0.08838834764831845f;
constexpr float LOG2E = 1.4426950408889634f;
constexpr float C2 = SCALE * LOG2E;
constexpr int PSTEP = 2 * NWG * BS;
constexpr int OOB = 0x40000000;
constexpr int CODE_BYTES = 7168;
constexpr int WS_VAL = 4096;
constexpr int WS_BYTES = WS_VAL + H * 4 * NWG * 128;
constexpr int AUX_NT = 2, AUX_SC1 = 16;

typedef unsigned v4u __attribute__((ext_vector_type(4)));
typedef float f32x4 __attribute__((ext_vector_type(4)));
typedef __bf16 bf16x8 __attribute__((ext_vector_type(8)));
typedef __bf16 bf16x2 __attribute__((ext_vector_type(2)));
typedef float f32x2 __attribute__((ext_vector_type(2)));
#define LAS __attribute__((address_space(3)))
#define CAS __attribute__((address_space(4)))

__device__ __forceinline__ f32x4 mfma(v4u a, v4u b, f32x4 c) {
  return __builtin_amdgcn_mfma_f32_16x16x32_bf16(__builtin_bit_cast(bf16x8, a), __builtin_bit_cast(bf16x8, b), c, 0, 0, 0);
}
__device__ __forceinline__ unsigned cvtpk(float lo, float hi) {
  f32x2 v = {lo, hi};
  bf16x2 r = __builtin_convertvector(v, bf16x2);
  return __builtin_bit_cast(unsigned, r);
}
template <int CTRL> __device__ __forceinline__ float dpp(float x) {
  return __builtin_bit_cast(float, __builtin_amdgcn_mov_dpp(__builtin_bit_cast(int, x), CTRL, 0xf, 0xf, true));
}
__device__ __forceinline__ float xg_max(float x) {
  auto s = __builtin_amdgcn_permlane16_swap(__float_as_uint(x), __float_as_uint(x), false, false);
  x = fmaxf(__uint_as_float(s[0]), __uint_as_float(s[1]));
  auto t = __builtin_amdgcn_permlane32_swap(__float_as_uint(x), __float_as_uint(x), false, false);
  return fmaxf(__uint_as_float(t[0]), __uint_as_float(t[1]));
}
__device__ __forceinline__ float xg_sum(float x) {
  auto s = __builtin_amdgcn_permlane16_swap(__float_as_uint(x), __float_as_uint(x), false, false);
  x = __uint_as_float(s[0]) + __uint_as_float(s[1]);
  auto t = __builtin_amdgcn_permlane32_swap(__float_as_uint(x), __float_as_uint(x), false, false);
  return __uint_as_float(t[0]) + __uint_as_float(t[1]);
}
template <int R> __device__ __forceinline__ float rowb1(float x) {
  int t = __builtin_amdgcn_update_dpp(0, __builtin_bit_cast(int, x), 0x150 + R, 0x5, 0xf, false);
  t = __builtin_amdgcn_update_dpp(t, __builtin_bit_cast(int, x), 0x150 + 4 + R, 0xa, 0xf, false);
  return __builtin_bit_cast(float, t);
}
__device__ __forceinline__ f32x4 rowb(float x) { return f32x4{rowb1<0>(x), rowb1<1>(x), rowb1<2>(x), rowb1<3>(x)}; }

struct Page { v4u r[4]; };

__device__ __forceinline__ Page load_k(__amdgpu_buffer_rsrc_t rs, int so, int vo) {
  Page p;
  p.r[0] = __builtin_amdgcn_raw_buffer_load_b128(rs, vo, so, AUX_NT);
  p.r[1] = __builtin_amdgcn_raw_buffer_load_b128(rs, vo + 128, so, AUX_NT);
  p.r[2] = __builtin_amdgcn_raw_buffer_load_b128(rs, vo, so + 8192, AUX_NT);
  p.r[3] = __builtin_amdgcn_raw_buffer_load_b128(rs, vo + 128, so + 8192, AUX_NT);
  return p;
}
__device__ __forceinline__ Page load_v(__amdgpu_buffer_rsrc_t rs, int so, int vo) {
  Page p;
  p.r[0] = __builtin_amdgcn_raw_buffer_load_b128(rs, vo, so, AUX_NT);
  p.r[1] = __builtin_amdgcn_raw_buffer_load_b128(rs, vo + 1024, so, AUX_NT);
  p.r[2] = __builtin_amdgcn_raw_buffer_load_b128(rs, vo + 2048, so, AUX_NT);
  p.r[3] = __builtin_amdgcn_raw_buffer_load_b128(rs, vo + 3072, so, AUX_NT);
  return p;
}

struct QFrag { v4u q[2]; };
__device__ __forceinline__ f32x4 qk_page(const Page& k, const QFrag& q) {
  const f32x4 z = {0.f, 0.f, 0.f, 0.f};
  f32x4 d0 = mfma(k.r[0], q.q[0], z);
  f32x4 d1 = mfma(k.r[2], q.q[0], z);
  d0 = mfma(k.r[1], q.q[1], d0);
  d1 = mfma(k.r[3], q.q[1], d1);
  f32x4 x;
#pragma unroll
  for (int r = 0; r < 4; ++r) {
    auto s = __builtin_amdgcn_permlane32_swap(__float_as_uint(d0[r]), __float_as_uint(d1[r]), false, false);
    x[r] = __uint_as_float(s[0]) + dpp<0x128>(__uint_as_float(s[1]));
  }
  return x;
}

struct State { float m, l, lse; f32x4 acc[8]; };

template <bool LAST>
__device__ __forceinline__ v4u softmax_step(State& st, f32x4 xa, f32x4 xb, int rema, int g4, bool fin) {
  if (LAST && rema - PSTEP < BS) {
    const int lima = rema - g4, limb = lima - PSTEP;
#pragma unroll
    for (int r = 0; r < 4; ++r) {
      xa[r] = (r < lima) ? xa[r] : -INFINITY;
      xb[r] = (r < limb) ? xb[r] : -INFINITY;
    }
  }
  float mx = fmaxf(fmaxf(fmaxf(xa[0], xa[1]), fmaxf(xa[2], xa[3])), fmaxf(fmaxf(xb[0], xb[1]), fmaxf(xb[2], xb[3])));
  mx = xg_max(mx);
  const float mn = fmaxf(st.m, mx);
  const float mc = (LAST && mn == -INFINITY) ? 0.f : mn * C2;
  float pa[4], pb[4];
#pragma unroll
  for (int r = 0; r < 4; ++r) {
    pa[r] = __builtin_amdgcn_exp2f(fmaf(xa[r], C2, -mc));
    pb[r] = __builtin_amdgcn_exp2f(fmaf(xb[r], C2, -mc));
  }
  const float ps = ((pa[0] + pa[1]) + (pa[2] + pa[3])) + ((pb[0] + pb[1]) + (pb[2] + pb[3]));
  float alpha = __builtin_amdgcn_exp2f(fmaf(st.m, C2, -mc));
  st.l = fmaf(st.l, alpha, ps);
  if (LAST) {
    st.l = xg_sum(st.l);
    const float inv = fin ? (st.l > 0.f ? __builtin_amdgcn_rcpf(st.l) : 0.f) : 1.f;
    st.lse = mc + __builtin_amdgcn_logf(st.l);
    alpha *= inv;
#pragma unroll
    for (int r = 0; r < 4; ++r) { pa[r] *= inv; pb[r] *= inv; }
  }
  const f32x4 av = rowb(alpha);
#pragma unroll
  for (int m = 0; m < 8; ++m) st.acc[m] *= av;
  st.m = mn;
  v4u p;
  p[0] = cvtpk(pa[0], pa[1]);
  p[1] = cvtpk(pa[2], pa[3]);
  p[2] = cvtpk(pb[0], pb[1]);
  p[3] = cvtpk(pb[2], pb[3]);
  return p;
}

__device__ __forceinline__ void pv_step(State& st, const Page& va, const Page& vb, v4u p) {
#pragma unroll
  for (int m = 0; m < 8; ++m) {
    const int w = m >> 1;
    const unsigned sel = (m & 1) ? 0x07060302u : 0x05040100u;
    v4u f;
    f[0] = __builtin_amdgcn_perm(va.r[1][w], va.r[0][w], sel);
    f[1] = __builtin_amdgcn_perm(va.r[3][w], va.r[2][w], sel);
    f[2] = __builtin_amdgcn_perm(vb.r[1][w], vb.r[0][w], sel);
    f[3] = __builtin_amdgcn_perm(vb.r[3][w], vb.r[2][w], sel);
    st.acc[m] = mfma(p, f, st.acc[m]);
  }
}

constexpr int REC = 8 * 32 * 4 + 16;
constexpr int LDS_CTL = HKV * REC;
constexpr int LDS_FIN = LDS_CTL + 8;
constexpr int FIN_ROW = 36;
constexpr int LDS_WORDS = LDS_FIN + 8 * FIN_ROW;

__device__ __forceinline__ void merge_record(State& st, const float* rec, int lane, int g) {
  f32x4 o[8];
#pragma unroll
  for (int m = 0; m < 8; ++m) o[m] = *(const f32x4*)(rec + (m * 32 + (lane & 31)) * 4);
  const f32x2 ml = *(const f32x2*)(rec + 1024 + 2 * (lane & 7));
  const float M = fmaxf(st.m, ml[0]);
  const float Mc = (M == -INFINITY) ? 0.f : M * C2;
  const float e1 = __builtin_amdgcn_exp2f(fmaf(st.m, C2, -Mc)), e2 = __builtin_amdgcn_exp2f(fmaf(ml[0], C2, -Mc));
  st.l = fmaf(st.l, e1, g == 0 ? e2 * ml[1] : 0.f);
  st.m = M;
  const f32x4 v1 = rowb(e1), v2 = rowb(e2);
#pragma unroll
  for (int m = 0; m < 8; ++m) st.acc[m] = st.acc[m] * v1 + o[m] * v2;
}

#define SB() do { asm volatile("" ::: "memory"); __builtin_amdgcn_sched_barrier(0); } while (0)
#define CB() asm volatile("" ::: "memory")
#define LDS_ADD(p, v) __hip_atomic_fetch_add((LAS unsigned*)(p), (v), __ATOMIC_RELAXED, __HIP_MEMORY_SCOPE_WORKGROUP)

__global__ void __launch_bounds__(NTHR, 2)
attn_decode(const char* q, const char* kc, const char* vc, const int* bt, const int* ctx_lens,
            char* ws, char* out, int kv_bytes, int pad_) {
  __shared__ __attribute__((aligned(16))) float lds[LDS_WORDS];
  const int tid = threadIdx.x, lane = tid & 63;
  const __amdgpu_buffer_rsrc_t rc =
      __builtin_amdgcn_make_buffer_rsrc((void*)(__builtin_amdgcn_s_getpc() & ~127ull), 0, CODE_BYTES, 0x00020000);
  const unsigned cpf = __builtin_amdgcn_raw_buffer_load_b32(rc, tid * 128, 0, 0);
  SB();
  const int w = __builtin_amdgcn_readfirstlane(tid >> 6);
  const int kvh = w & 3, sub = w >> 2, c = blockIdx.x;
  const int n = lane & 15, g = lane >> 4;
  asm volatile("" :: "s"(q), "s"(kc), "s"(vc), "s"(bt), "s"(ctx_lens), "s"(ws), "s"(out), "s"(kv_bytes));

  const int pc = (c + 64 * kvh) & (NWG - 1);
  const __amdgpu_buffer_rsrc_t rb = __builtin_amdgcn_make_buffer_rsrc((void*)bt, 0, NB * 4, 0x00020000);
  const int physv = __builtin_amdgcn_raw_buffer_load_b32(rb, lane * (2 * NWG * 4), (sub * NWG + pc) * 4, 0);
  int ctx = *(const CAS int*)ctx_lens;
  const unsigned ep = *(const CAS unsigned*)ws;
  volatile LAS unsigned* ctl = (volatile LAS unsigned*)(lds + LDS_CTL);
  if (tid < 8) ctl[tid] = 0u;
  if (sub == 0) __builtin_amdgcn_s_setprio(1);

  QFrag qf;
  const char* qrow = q + (kvh * G + (n & 7)) * (D * 2) + (n >> 3) * 64 + g * 16;
  qf.q[0] = *(const v4u*)(qrow);
  qf.q[1] = *(const v4u*)(qrow + 128);
  const int koff = (n & 7) * 1024 + (n >> 3) * 64 + g * 16;
  const int voff = g * 4096 + n * 16;
  const __amdgpu_buffer_rsrc_t rk = __builtin_amdgcn_make_buffer_rsrc((void*)(kc + kvh * 256), 0, kv_bytes - kvh * 256, 0x00020000);
  const __amdgpu_buffer_rsrc_t rv = __builtin_amdgcn_make_buffer_rsrc((void*)(vc + kvh * 256), 0, kv_bytes - kvh * 256, 0x00020000);
  State st;
  st.m = -INFINITY; st.l = 0.f; st.lse = -INFINITY;
#pragma unroll
  for (int m = 0; m < 8; ++m) st.acc[m] = f32x4{0.f, 0.f, 0.f, 0.f};
  float* rec = lds + kvh * REC;

  asm volatile("s_waitcnt lgkmcnt(0)" :: "s"(ctx), "s"(ep) : "memory");
  __builtin_amdgcn_s_barrier();
  SB();
  ctx = ctx < 0 ? 0 : (ctx > LK ? LK : ctx);
  int rem = ctx - (sub * NWG + pc) * BS;
  const int sov = (rem - lane * PSTEP > 0) ? (physv & 0xffff) << 14 : OOB;

  int s0 = __builtin_amdgcn_readlane(sov, 0), s1 = __builtin_amdgcn_readlane(sov, 1);
  Page KA = load_k(rk, s0, koff), KB = load_k(rk, s1, koff);
  Page VA = load_v(rv, s0, voff), VB = load_v(rv, s1, voff);
  SB();
#pragma clang loop unroll(disable)
  for (int i = 2; rem - 2 * PSTEP > 0; i += 2, rem -= 2 * PSTEP) {
    s0 = __builtin_amdgcn_readlane(sov, i); s1 = __builtin_amdgcn_readlane(sov, i + 1);
    const f32x4 xa = qk_page(KA, qf), xb = qk_page(KB, qf);
    SB();
    KA = load_k(rk, s0, koff); KB = load_k(rk, s1, koff);
    SB();
    const v4u p = softmax_step<false>(st, xa, xb, 0, 0, false);
    SB();
    pv_step(st, VA, VB, p);
    SB();
    VA = load_v(rv, s0, voff); VB = load_v(rv, s1, voff);
    SB();
  }
  if (sub) {
    while (__builtin_amdgcn_readfirstlane(ctl[kvh]) == 0u) __builtin_amdgcn_s_sleep(1);
    CB();
    merge_record(st, rec, lane, g);
  }
  SB();
  const unsigned e1_ = ep + 1u, hA = e1_ * 0x9E3779B1u, KX = (e1_ << 1) | 1u, hB = hA ^ KX;
  v4u ta, tb;
  int pvo = WS_VAL + ((((kvh * G + 4 * (g & 1)) * 4 + (n >> 2)) * NWG + c) * 8 + 2 * (n & 3) + (g >> 1)) * 16;
  {
    const f32x4 xa = qk_page(KA, qf), xb = qk_page(KB, qf);
    const v4u p = softmax_step<true>(st, xa, xb, rem, 4 * g, sub != 0);
    const f32x4 lb = rowb(st.lse);
#pragma unroll
    for (int r = 0; r < 4; ++r) { ta[r] = __float_as_uint(lb[r]) ^ hA; tb[r] = __float_as_uint(lb[r]) ^ hB; }
    asm volatile("" : "+v"(ta), "+v"(tb), "+v"(pvo));
    SB();
    pv_step(st, VA, VB, p);
  }
  asm volatile("" :: "v"(cpf));
  if (sub) {
    const __amdgpu_buffer_rsrc_t rw = __builtin_amdgcn_make_buffer_rsrc((void*)ws, 0, WS_BYTES, 0x00020000);
#pragma unroll
    for (int r = 0; r < 4; ++r) {
      const unsigned w0 = cvtpk(st.acc[0][r], st.acc[1][r]), w1 = cvtpk(st.acc[2][r], st.acc[3][r]);
      const unsigned w2 = cvtpk(st.acc[4][r], st.acc[5][r]), w3 = cvtpk(st.acc[6][r], st.acc[7][r]);
      const auto x0 = __builtin_amdgcn_permlane32_swap(w0, w2, false, false);
      const auto x1 = __builtin_amdgcn_permlane32_swap(w1, w3, false, false);
      const v4u gv = {x0[0], ta[r], x1[0], tb[r]};
      __builtin_amdgcn_raw_buffer_store_b128(gv, rw, pvo + r * (4 * NWG * 128), 0, AUX_SC1);
    }
    if (lane == 0) LDS_ADD(ctl + 4, 1u);
  } else {
    if (lane < 32) {
#pragma unroll
      for (int m = 0; m < 8; ++m) *(f32x4*)(rec + (m * 32 + lane) * 4) = st.acc[m];
      if (lane < G) *(f32x2*)(rec + 1024 + 2 * lane) = f32x2{st.m, st.l};
    }
    CB();
    if (lane == 0) ctl[kvh] = 1u;
    __builtin_amdgcn_s_setprio(0);
  }
  if (c & 1) return;

  const int h = c >> 3, u = (c >> 1) & 3;
  const int vo = WS_VAL + ((c >> 1) * NWG + 32 * w) * 128 + lane * 16;
  const v4u rdesc = {(unsigned)(uintptr_t)ws, (unsigned)((uintptr_t)ws >> 32), (unsigned)WS_BYTES, 0x00020000u};
  while (__builtin_amdgcn_readfirstlane(ctl[4]) < 4u) __builtin_amdgcn_s_sleep(1);
  __builtin_amdgcn_s_sleep(10);
  unsigned cnt = 1u << 20;
  v4u gr[4];
  {
    unsigned tt[4], tu[4];
    unsigned long long mm[4], mx0, mx1;
    asm volatile(
      "s_nop 4\n\t"
      "buffer_load_dwordx4 v[128:131], %[vo], %[rs], 0 offen sc1\n\t"
      "buffer_load_dwordx4 v[132:135], %[vo], %[rs], 0 offen offset:1024 sc1\n\t"
      "buffer_load_dwordx4 v[136:139], %[vo], %[rs], 0 offen offset:2048 sc1\n\t"
      "buffer_load_dwordx4 v[140:143], %[vo], %[rs], 0 offen offset:3072 sc1\n\t"
      "1:\n\t"
      "s_waitcnt vmcnt(0)\n\t"
      "v_xor_b32 %[t0], v129, v131\n\t"
      "v_xor_b32 %[t1], v133, v135\n\t"
      "v_xor_b32 %[t2], v137, v139\n\t"
      "v_xor_b32 %[t3], v141, v143\n\t"
      "v_xor_b32_dpp %[u0], v129, v129 quad_perm:[1,0,3,2] row_mask:0xf bank_mask:0xf\n\t"
      "v_xor_b32_dpp %[u1], v133, v133 quad_perm:[1,0,3,2] row_mask:0xf bank_mask:0xf\n\t"
      "v_xor_b32_dpp %[u2], v137, v137 quad_perm:[1,0,3,2] row_mask:0xf bank_mask:0xf\n\t"
      "v_xor_b32_dpp %[u3], v141, v141 quad_perm:[1,0,3,2] row_mask:0xf bank_mask:0xf\n\t"
      "v_bitop3_b32 %[t0], %[t0], %[kx], %[u0] bitop3:0xbe\n\t"
      "v_bitop3_b32 %[t1], %[t1], %[kx], %[u1] bitop3:0xbe\n\t"
      "v_bitop3_b32 %[t2], %[t2], %[kx], %[u2] bitop3:0xbe\n\t"
      "v_bitop3_b32 %[t3], %[t3], %[kx], %[u3] bitop3:0xbe\n\t"
      "v_cmp_ne_u32 %[m0], 0, %[t0]\n\t"
      "v_cmp_ne_u32 %[m1], 0, %[t1]\n\t"
      "v_cmp_ne_u32 %[m2], 0, %[t2]\n\t"
      "v_cmp_ne_u32 %[m3], 0, %[t3]\n\t"
      "s_or_b64 %[x0], %[m0], %[m1]\n\t"
      "s_or_b64 %[x1], %[m2], %[m3]\n\t"
      "s_or_b64 %[x0], %[x0], %[x1]\n\t"
      "s_cbranch_scc0 2f\n\t"
      "s_sub_u32 %[cnt], %[cnt], 1\n\t"
      "s_cbranch_scc1 2f\n\t"
      "s_sleep 2\n\t"
      "s_mov_b64 exec, %[m0]\n\t"
      "buffer_load_dwordx4 v[128:131], %[vo], %[rs], 0 offen sc1\n\t"
      "s_mov_b64 exec, %[m1]\n\t"
      "buffer_load_dwordx4 v[132:135], %[vo], %[rs], 0 offen offset:1024 sc1\n\t"
      "s_mov_b64 exec, %[m2]\n\t"
      "buffer_load_dwordx4 v[136:139], %[vo], %[rs], 0 offen offset:2048 sc1\n\t"
      "s_mov_b64 exec, %[m3]\n\t"
      "buffer_load_dwordx4 v[140:143], %[vo], %[rs], 0 offen offset:3072 sc1\n\t"
      "s_mov_b64 exec, -1\n\t"
      "s_branch 1b\n\t"
      "2:\n\t"
      : "={v[128:131]}"(gr[0]), "={v[132:135]}"(gr[1]), "={v[136:139]}"(gr[2]), "={v[140:143]}"(gr[3]),
        [t0] "=&v"(tt[0]), [t1] "=&v"(tt[1]), [t2] "=&v"(tt[2]), [t3] "=&v"(tt[3]),
        [u0] "=&v"(tu[0]), [u1] "=&v"(tu[1]), [u2] "=&v"(tu[2]), [u3] "=&v"(tu[3]),
        [m0] "=&s"(mm[0]), [m1] "=&s"(mm[1]), [m2] "=&s"(mm[2]), [m3] "=&s"(mm[3]),
        [x0] "=&s"(mx0), [x1] "=&s"(mx1), [cnt] "+s"(cnt)
      : [vo] "v"(vo), [rs] "s"(rdesc), [kx] "s"(KX)
      : "memory", "scc");
  }
  if (cnt == 0xffffffffu) return;
  float gl[4];
#pragma unroll
  for (int i = 0; i < 4; ++i) gl[i] = __uint_as_float(gr[i][1] ^ hA);
  float M = fmaxf(fmaxf(gl[0], gl[1]), fmaxf(gl[2], gl[3]));
  M = xg_max(fmaxf(M, dpp<0x128>(M)));
  const float Ms = (M == -INFINITY) ? 0.f : M;
  float W = 0.f, a0 = 0.f, a1 = 0.f, a2 = 0.f, a3 = 0.f;
#pragma unroll
  for (int i = 0; i < 4; ++i) {
    const float e = __builtin_amdgcn_exp2f(gl[i] - Ms);
    W += e;
    a0 = fmaf(e, __uint_as_float(gr[i][0] << 16), a0); a1 = fmaf(e, __uint_as_float(gr[i][0] & 0xffff0000u), a1);
    a2 = fmaf(e, __uint_as_float(gr[i][2] << 16), a2); a3 = fmaf(e, __uint_as_float(gr[i][2] & 0xffff0000u), a3);
  }
  const auto s01 = __builtin_amdgcn_permlane32_swap(__float_as_uint(a0), __float_as_uint(a1), false, false);
  const auto s23 = __builtin_amdgcn_permlane32_swap(__float_as_uint(a2), __float_as_uint(a3), false, false);
  const float u01 = __uint_as_float(s01[0]) + __uint_as_float(s01[1]);
  const float u23 = __uint_as_float(s23[0]) + __uint_as_float(s23[1]);
  const auto sx = __builtin_amdgcn_permlane16_swap(__float_as_uint(u01), __float_as_uint(u23), false, false);
  float v = __uint_as_float(sx[0]) + __uint_as_float(sx[1]);
  v += dpp<0x128>(v);
  W = xg_sum(W + dpp<0x128>(W));
  float* slot = lds + LDS_FIN + w * FIN_ROW;
  unsigned tk = 0;
  if (n < 8) {
    slot[4 * n + 2 * (g & 1) + (g >> 1)] = v;
    if (lane == 0) {
      *(f32x2*)(slot + 32) = f32x2{M, W};
      CB();
      int ci = 5;
      asm volatile("" : "+v"(ci));
      tk = LDS_ADD(ctl + ci, 1u);
    }
  }
  CB();
  const float* fin = lds + LDS_FIN;
  const int dd = lane & 31;
  float mw[8], ww[8], av[8];
#pragma unroll
  for (int i = 0; i < 8; ++i) {
    const f32x2 x = *(const f32x2*)(fin + i * FIN_ROW + 32);
    mw[i] = x[0]; ww[i] = x[1]; av[i] = fin[i * FIN_ROW + dd];
  }
  CB();
  if (__builtin_amdgcn_readfirstlane(tk) != 7u) return;
  M = fmaxf(fmaxf(fmaxf(mw[0], mw[1]), fmaxf(mw[2], mw[3])), fmaxf(fmaxf(mw[4], mw[5]), fmaxf(mw[6], mw[7])));
  const float Mf = (M == -INFINITY) ? 0.f : M;
  float Wt = 0.f, at = 0.f;
#pragma unroll
  for (int i = 0; i < 8; ++i) {
    const float fi = __builtin_amdgcn_exp2f(mw[i] - Mf);
    Wt = fmaf(fi, ww[i], Wt); at = fmaf(fi, av[i], at);
  }
  if (__builtin_expect(ctx < LK && !(M > -1.0e9f * LOG2E + 2048.f), 0)) {
    const float lm = -1.0e9f * LOG2E;
    const float Mall = fmaxf(M, lm);
    const float wv = (M == -INFINITY) ? 0.f : __builtin_amdgcn_exp2f(M - Mall);
    const float wm = __builtin_amdgcn_exp2f(lm - Mall);
    float sv = 0.f;
    const int hk = h >> 3;
    for (int t = ctx + (lane >> 5); t < LK; t += 2) {
      const unsigned short d = *(const unsigned short*)(vc + ((size_t)bt[t / BS] * BS + (t % BS)) * (HKV * D * 2) + hk * (D * 2) + (u * 32 + dd) * 2);
      sv += __uint_as_float((unsigned)d << 16);
    }
    sv += __shfl_xor(sv, 32);
    at = at * wv + sv * wm;
    Wt = Wt * wv + wm * (float)(LK - ctx);
  }
  const float res = at * __builtin_amdgcn_rcpf(Wt);
  const unsigned pk = cvtpk(res, dpp<0xB1>(res));
  const __amdgpu_buffer_rsrc_t ro = __builtin_amdgcn_make_buffer_rsrc((void*)out, 0, H * D * 2, 0x00020000);
  __builtin_amdgcn_raw_buffer_store_b32(pk, ro, (lane < 32 && !(lane & 1)) ? (h * D + u * 32 + lane) * 2 : OOB, 0, 0);
  if (c == 0 && lane == 0) __hip_atomic_store((unsigned*)ws, e1_, __ATOMIC_RELAXED, __HIP_MEMORY_SCOPE_AGENT);
}

}

extern "C" void kernel_launch(void* const* d_in, const int* in_sizes, int n_in, void* d_out, int out_size, void* d_ws,
                              size_t ws_size, hipStream_t stream) {
  attn_decode<<<NWG, NTHR, 0, stream>>>((const char*)d_in[0], (const char*)d_in[1], (const char*)d_in[2], (const int*)d_in[3],
                                        (const int*)d_in[4], (char*)d_ws, (char*)d_out, in_sizes[1] * 2, 0);
}
```

```cpp
#include <hip/hip_runtime.h>
#include <hip/hip_bf16.h>
#include <stdint.h>

namespace {

constexpr int H = 32, HKV = 4, D = 128, BS = 16, NB = 4096, G = H / HKV;
constexpr int LK = NB * BS;
constexpr int NWG = 256, NWAVE = 8, NTHR = NWAVE * 64;
constexpr float SCALE = 0.08838834764831845f;
constexpr float LOG2E = 1.4426950408889634f;
constexpr float C2 = SCALE * LOG2E;
constexpr int PSTEP = 2 * NWG * BS;
constexpr int OOB = 0x40000000;
constexpr int CODE_BYTES = 7168;
constexpr int WS_VAL = 4096;
constexpr int WS_BYTES = WS_VAL + H * 4 * NWG * 128;
constexpr int AUX_NT = 2, AUX_SC1 = 16;

typedef unsigned v4u __attribute__((ext_vector_type(4)));
typedef float f32x4 __attribute__((ext_vector_type(4)));
typedef __bf16 bf16x8 __attribute__((ext_vector_type(8)));
typedef __bf16 bf16x2 __attribute__((ext_vector_type(2)));
typedef float f32x2 __attribute__((ext_vector_type(2)));
#define LAS __attribute__((address_space(3)))
#define CAS __attribute__((address_space(4)))

__device__ __forceinline__ f32x4 mfma(v4u a, v4u b, f32x4 c) {
  return __builtin_amdgcn_mfma_f32_16x16x32_bf16(__builtin_bit_cast(bf16x8, a), __builtin_bit_cast(bf16x8, b), c, 0, 0, 0);
}
__device__ __forceinline__ unsigned cvtpk(float lo, float hi) {
  f32x2 v = {lo, hi};
  bf16x2 r = __builtin_convertvector(v, bf16x2);
  return __builtin_bit_cast(unsigned, r);
}
template <int CTRL> __device__ __forceinline__ float dpp(float x) {
  return __builtin_bit_cast(float, __builtin_amdgcn_mov_dpp(__builtin_bit_cast(int, x), CTRL, 0xf, 0xf, true));
}
__device__ __forceinline__ float xg_max(float x) {
  auto s = __builtin_amdgcn_permlane16_swap(__float_as_uint(x), __float_as_uint(x), false, false);
  x = fmaxf(__uint_as_float(s[0]), __uint_as_float(s[1]));
  auto t = __builtin_amdgcn_permlane32_swap(__float_as_uint(x), __float_as_uint(x), false, false);
  return fmaxf(__uint_as_float(t[0]), __uint_as_float(t[1]));
}
__device__ __forceinline__ float xg_sum(float x) {
  auto s = __builtin_amdgcn_permlane16_swap(__float_as_uint(x), __float_as_uint(x), false, false);
  x = __uint_as_float(s[0]) + __uint_as_float(s[1]);
  auto t = __builtin_amdgcn_permlane32_swap(__float_as_uint(x), __float_as_uint(x), false, false);
  return __uint_as_float(t[0]) + __uint_as_float(t[1]);
}
template <int R> __device__ __forceinline__ float rowb1(float x) {
  int t = __builtin_amdgcn_update_dpp(0, __builtin_bit_cast(int, x), 0x150 + R, 0x5, 0xf, false);
  t = __builtin_amdgcn_update_dpp(t, __builtin_bit_cast(int, x), 0x150 + 4 + R, 0xa, 0xf, false);
  return __builtin_bit_cast(float, t);
}
__device__ __forceinline__ f32x4 rowb(float x) { return f32x4{rowb1<0>(x), rowb1<1>(x), rowb1<2>(x), rowb1<3>(x)}; }

struct Page { v4u r[4]; };

__device__ __forceinline__ Page load_k(__amdgpu_buffer_rsrc_t rs, int so, int vo) {
  Page p;
  p.r[0] = __builtin_amdgcn_raw_buffer_load_b128(rs, vo, so, AUX_NT);
  p.r[1] = __builtin_amdgcn_raw_buffer_load_b128(rs, vo + 128, so, AUX_NT);
  p.r[2] = __builtin_amdgcn_raw_buffer_load_b128(rs, vo, so + 8192, AUX_NT);
  p.r[3] = __builtin_amdgcn_raw_buffer_load_b128(rs, vo + 128, so + 8192, AUX_NT);
  return p;
}
__device__ __forceinline__ Page load_v(__amdgpu_buffer_rsrc_t rs, int so, int vo) {
  Page p;
  p.r[0] = __builtin_amdgcn_raw_buffer_load_b128(rs, vo, so, AUX_NT);
  p.r[1] = __builtin_amdgcn_raw_buffer_load_b128(rs, vo + 1024, so, AUX_NT);
  p.r[2] = __builtin_amdgcn_raw_buffer_load_b128(rs, vo + 2048, so, AUX_NT);
  p.r[3] = __builtin_amdgcn_raw_buffer_load_b128(rs, vo + 3072, so, AUX_NT);
  return p;
}

struct QFrag { v4u q[2]; };
__device__ __forceinline__ f32x4 qk_page(const Page& k, const QFrag& q) {
  const f32x4 z = {0.f, 0.f, 0.f, 0.f};
  f32x4 d0 = mfma(k.r[0], q.q[0], z);
  f32x4 d1 = mfma(k.r[2], q.q[0], z);
  d0 = mfma(k.r[1], q.q[1], d0);
  d1 = mfma(k.r[3], q.q[1], d1);
  f32x4 x;
#pragma unroll
  for (int r = 0; r < 4; ++r) {
    auto s = __builtin_amdgcn_permlane32_swap(__float_as_uint(d0[r]), __float_as_uint(d1[r]), false, false);
    x[r] = __uint_as_float(s[0]) + dpp<0x128>(__uint_as_float(s[1]));
  }
  return x;
}

struct State { float m, l, lse; f32x4 acc[8]; };

template <bool LAST>
__device__ __forceinline__ v4u softmax_step(State& st, f32x4 xa, f32x4 xb, int rema, int g4, bool fin) {
  if (LAST && rema - PSTEP < BS) {
    const int lima = rema - g4, limb = lima - PSTEP;
#pragma unroll
    for (int r = 0; r < 4; ++r) {
      xa[r] = (r < lima) ? xa[r] : -INFINITY;
      xb[r] = (r < limb) ? xb[r] : -INFINITY;
    }
  }
  float mx = fmaxf(fmaxf(fmaxf(xa[0], xa[1]), fmaxf(xa[2], xa[3])), fmaxf(fmaxf(xb[0], xb[1]), fmaxf(xb[2], xb[3])));
  mx = xg_max(mx);
  const float mn = fmaxf(st.m, mx);
  const float mc = (LAST && mn == -INFINITY) ? 0.f : mn * C2;
  float pa[4], pb[4];
#pragma unroll
  for (int r = 0; r < 4; ++r) {
    pa[r] = __builtin_amdgcn_exp2f(fmaf(xa[r], C2, -mc));
    pb[r] = __builtin_amdgcn_exp2f(fmaf(xb[r], C2, -mc));
  }
  const float ps = ((pa[0] + pa[1]) + (pa[2] + pa[3])) + ((pb[0] + pb[1]) + (pb[2] + pb[3]));
  float alpha = __builtin_amdgcn_exp2f(fmaf(st.m, C2, -mc));
  st.l = fmaf(st.l, alpha, ps);
  if (LAST) {
    st.l = xg_sum(st.l);
    const float inv = fin ? (st.l > 0.f ? __builtin_amdgcn_rcpf(st.l) : 0.f) : 1.f;
    st.lse = mc + __builtin_amdgcn_logf(st.l);
    alpha *= inv;
#pragma unroll
    for (int r = 0; r < 4; ++r) { pa[r] *= inv; pb[r] *= inv; }
  }
  const f32x4 av = rowb(alpha);
#pragma unroll
  for (int m = 0; m < 8; ++m) st.acc[m] *= av;
  st.m = mn;
  v4u p;
  p[0] = cvtpk(pa[0], pa[1]);
  p[1] = cvtpk(pa[2], pa[3]);
  p[2] = cvtpk(pb[0], pb[1]);
  p[3] = cvtpk(pb[2], pb[3]);
  return p;
}

__device__ __forceinline__ void pv_step(State& st, const Page& va, const Page& vb, v4u p) {
#pragma unroll
  for (int m = 0; m < 8; ++m) {
    const int w = m >> 1;
    const unsigned sel = (m & 1) ? 0x07060302u : 0x05040100u;
    v4u f;
    f[0] = __builtin_amdgcn_perm(va.r[1][w], va.r[0][w], sel);
    f[1] = __builtin_amdgcn_perm(va.r[3][w], va.r[2][w], sel);
    f[2] = __builtin_amdgcn_perm(vb.r[1][w], vb.r[0][w], sel);
    f[3] = __builtin_amdgcn_perm(vb.r[3][w], vb.r[2][w], sel);
    st.acc[m] = mfma(p, f, st.acc[m]);
  }
}

constexpr int REC = 8 * 32 * 4 + 16;
constexpr int LDS_CTL = HKV * REC;
constexpr int LDS_FIN = LDS_CTL + 8;
constexpr int FIN_ROW = 36;
constexpr int LDS_WORDS = LDS_FIN + 8 * FIN_ROW;

__device__ __forceinline__ void merge_record(State& st, const float* rec, int lane, int g) {
  f32x4 o[8];
#pragma unroll
  for (int m = 0; m < 8; ++m) o[m] = *(const f32x4*)(rec + (m * 32 + (lane & 31)) * 4);
  const f32x2 ml = *(const f32x2*)(rec + 1024 + 2 * (lane & 7));
  const float M = fmaxf(st.m, ml[0]);
  const float Mc = (M == -INFINITY) ? 0.f : M * C2;
  const float e1 = __builtin_amdgcn_exp2f(fmaf(st.m, C2, -Mc)), e2 = __builtin_amdgcn_exp2f(fmaf(ml[0], C2, -Mc));
  st.l = fmaf(st.l, e1, g == 0 ? e2 * ml[1] : 0.f);
  st.m = M;
  const f32x4 v1 = rowb(e1), v2 = rowb(e2);
#pragma unroll
  for (int m = 0; m < 8; ++m) st.acc[m] = st.acc[m] * v1 + o[m] * v2;
}

#define SB() do { asm volatile("" ::: "memory"); __builtin_amdgcn_sched_barrier(0); } while (0)
#define CB() asm volatile("" ::: "memory")
#define LDS_ADD(p, v) __hip_atomic_fetch_add((LAS unsigned*)(p), (v), __ATOMIC_RELAXED, __HIP_MEMORY_SCOPE_WORKGROUP)

__global__ void __launch_bounds__(NTHR, 2)
attn_decode(const char* q, const char* kc, const char* vc, const int* bt, const int* ctx_lens,
            char* ws, char* out, int kv_bytes, int pad_) {
  __shared__ __attribute__((aligned(16))) float lds[LDS_WORDS];
  const int tid = threadIdx.x, lane = tid & 63;
  const __amdgpu_buffer_rsrc_t rc =
      __builtin_amdgcn_make_buffer_rsrc((void*)(__builtin_amdgcn_s_getpc() & ~127ull), 0, CODE_BYTES, 0x00020000);
  const unsigned cpf = __builtin_amdgcn_raw_buffer_load_b32(rc, tid * 128, 0, 0);
  SB();
  const int w = __builtin_amdgcn_readfirstlane(tid >> 6);
  const int kvh = w & 3, sub = w >> 2, c = blockIdx.x;
  const int n = lane & 15, g = lane >> 4;
  asm volatile("" :: "s"(q), "s"(kc), "s"(vc), "s"(bt), "s"(ctx_lens), "s"(ws), "s"(out), "s"(kv_bytes));

  const int pc = (c + 64 * kvh) & (NWG - 1);
  const __amdgpu_buffer_rsrc_t rb = __builtin_amdgcn_make_buffer_rsrc((void*)bt, 0, NB * 4, 0x00020000);
  const int physv = __builtin_amdgcn_raw_buffer_load_b32(rb, lane * (2 * NWG * 4), (sub * NWG + pc) * 4, 0);
  unsigned tch = 0;
  if (w == 7) {
    const int gi = (c >> 3) * 1 + (lane >> 1);
    const char* tp = ((lane & 1) ? vc : kc) + (size_t)min(gi << 21, kv_bytes - 4);
    if (lane >= 2 * 1) tp = (c >> 3) < 3 ? ws + min(WS_VAL + ((c >> 3) << 21), WS_BYTES - 4) : out;
    if (lane <= 2 * 1) tch = *(const unsigned*)tp;
  }
  CB();
  int ctx = *(const CAS int*)ctx_lens;
  const unsigned ep = *(const CAS unsigned*)ws;
  volatile LAS unsigned* ctl = (volatile LAS unsigned*)(lds + LDS_CTL);
  if (tid < 8) ctl[tid] = 0u;
  if (sub == 0) __builtin_amdgcn_s_setprio(1);

  QFrag qf;
  const char* qrow = q + (kvh * G + (n & 7)) * (D * 2) + (n >> 3) * 64 + g * 16;
  qf.q[0] = *(const v4u*)(qrow);
  qf.q[1] = *(const v4u*)(qrow + 128);
  const int koff = (n & 7) * 1024 + (n >> 3) * 64 + g * 16;
  const int voff = g * 4096 + n * 16;
  const __amdgpu_buffer_rsrc_t rk = __builtin_amdgcn_make_buffer_rsrc((void*)(kc + kvh * 256), 0, kv_bytes - kvh * 256, 0x00020000);
  const __amdgpu_buffer_rsrc_t rv = __builtin_amdgcn_make_buffer_rsrc((void*)(vc + kvh * 256), 0, kv_bytes - kvh * 256, 0x00020000);
  State st;
  st.m = -INFINITY; st.l = 0.f; st.lse = -INFINITY;
#pragma unroll
  for (int m = 0; m < 8; ++m) st.acc[m] = f32x4{0.f, 0.f, 0.f, 0.f};
  float* rec = lds + kvh * REC;

  asm volatile("s_waitcnt lgkmcnt(0)" :: "s"(ctx), "s"(ep) : "memory");
  __builtin_amdgcn_s_barrier();
  SB();
  ctx = ctx < 0 ? 0 : (ctx > LK ? LK : ctx);
  int rem = ctx - (sub * NWG + pc) * BS;
  const int sov = (rem - lane * PSTEP > 0) ? (physv & 0xffff) << 14 : OOB;

  int s0 = __builtin_amdgcn_readlane(sov, 0), s1 = __builtin_amdgcn_readlane(sov, 1);
  Page KA = load_k(rk, s0, koff), KB = load_k(rk, s1, koff);
  Page VA = load_v(rv, s0, voff), VB = load_v(rv, s1, voff);
  SB();
#pragma clang loop unroll(disable)
  for (int i = 2; rem - 2 * PSTEP > 0; i += 2, rem -= 2 * PSTEP) {
    s0 = __builtin_amdgcn_readlane(sov, i); s1 = __builtin_amdgcn_readlane(sov, i + 1);
    const f32x4 xa = qk_page(KA, qf), xb = qk_page(KB, qf);
    SB();
    KA = load_k(rk, s0, koff); KB = load_k(rk, s1, koff);
    SB();
    const v4u p = softmax_step<false>(st, xa, xb, 0, 0, false);
    SB();
    pv_step(st, VA, VB, p);
    SB();
    VA = load_v(rv, s0, voff); VB = load_v(rv, s1, voff);
    SB();
  }
  if (sub) {
    while (__builtin_amdgcn_readfirstlane(ctl[kvh]) == 0u) __builtin_amdgcn_s_sleep(1);
    CB();
    merge_record(st, rec, lane, g);
  }
  SB();
  const unsigned e1_ = ep + 1u, hA = e1_ * 0x9E3779B1u, KX = (e1_ << 1) | 1u, hB = hA ^ KX;
  v4u ta, tb;
  int pvo = WS_VAL + ((((kvh * G + 4 * (g & 1)) * 4 + (n >> 2)) * NWG + c) * 8 + 2 * (n & 3) + (g >> 1)) * 16;
  {
    const f32x4 xa = qk_page(KA, qf), xb = qk_page(KB, qf);
    const v4u p = softmax_step<true>(st, xa, xb, rem, 4 * g, sub != 0);
    const f32x4 lb = rowb(st.lse);
#pragma unroll
    for (int r = 0; r < 4; ++r) { ta[r] = __float_as_uint(lb[r]) ^ hA; tb[r] = __float_as_uint(lb[r]) ^ hB; }
    asm volatile("" : "+v"(ta), "+v"(tb), "+v"(pvo));
    SB();
    pv_step(st, VA, VB, p);
  }
  asm volatile("" :: "v"(cpf), "v"(tch));
  if (sub) {
    const __amdgpu_buffer_rsrc_t rw = __builtin_amdgcn_make_buffer_rsrc((void*)ws, 0, WS_BYTES, 0x00020000);
#pragma unroll
    for (int r = 0; r < 4; ++r) {
      const unsigned w0 = cvtpk(st.acc[0][r], st.acc[1][r]), w1 = cvtpk(st.acc[2][r], st.acc[3][r]);
      const unsigned w2 = cvtpk(st.acc[4][r], st.acc[5][r]), w3 = cvtpk(st.acc[6][r], st.acc[7][r]);
      const auto x0 = __builtin_amdgcn_permlane32_swap(w0, w2, false, false);
      const auto x1 = __builtin_amdgcn_permlane32_swap(w1, w3, false, false);
      const v4u gv = {x0[0], ta[r], x1[0], tb[r]};
      __builtin_amdgcn_raw_buffer_store_b128(gv, rw, pvo + r * (4 * NWG * 128), 0, AUX_SC1);
    }
    if (lane == 0) LDS_ADD(ctl + 4, 1u);
  } else {
    if (lane < 32) {
#pragma unroll
      for (int m = 0; m < 8; ++m) *(f32x4*)(rec + (m * 32 + lane) * 4) = st.acc[m];
      if (lane < G) *(f32x2*)(rec + 1024 + 2 * lane) = f32x2{st.m, st.l};
    }
    CB();
    if (lane == 0) ctl[kvh] = 1u;
    __builtin_amdgcn_s_setprio(0);
  }
  if (c & 1) return;

  const int h = c >> 3, u = (c >> 1) & 3;
  const int vo = WS_VAL + ((c >> 1) * NWG + 32 * w) * 128 + lane * 16;
  const v4u rdesc = {(unsigned)(uintptr_t)ws, (unsigned)((uintptr_t)ws >> 32) & 0xffffu, (unsigned)WS_BYTES, 0x00020000u};
  while (__builtin_amdgcn_readfirstlane(ctl[4]) < 4u) __builtin_amdgcn_s_sleep(1);
  __builtin_amdgcn_s_sleep(10);
  unsigned cnt = 1u << 20;
  v4u gr[4];
  {
    unsigned tt[4], tu[4];
    unsigned long long mm[4], mx0, mx1;
    asm volatile(
      "s_nop 4\n\t"
      "buffer_load_dwordx4 v[128:131], %[vo], %[rs], 0 offen sc1\n\t"
      "buffer_load_dwordx4 v[132:135], %[vo], %[rs], 0 offen offset:1024 sc1\n\t"
      "buffer_load_dwordx4 v[136:139], %[vo], %[rs], 0 offen offset:2048 sc1\n\t"
      "buffer_load_dwordx4 v[140:143], %[vo], %[rs], 0 offen offset:3072 sc1\n\t"
      "1:\n\t"
      "s_waitcnt vmcnt(0)\n\t"
      "v_xor_b32 %[t0], v129, v131\n\t"
      "v_xor_b32 %[t1], v133, v135\n\t"
      "v_xor_b32 %[t2], v137, v139\n\t"
      "v_xor_b32 %[t3], v141, v143\n\t"
      "v_xor_b32_dpp %[u0], v129, v129 quad_perm:[1,0,3,2] row_mask:0xf bank_mask:0xf\n\t"
      "v_xor_b32_dpp %[u1], v133, v133 quad_perm:[1,0,3,2] row_mask:0xf bank_mask:0xf\n\t"
      "v_xor_b32_dpp %[u2], v137, v137 quad_perm:[1,0,3,2] row_mask:0xf bank_mask:0xf\n\t"
      "v_xor_b32_dpp %[u3], v141, v141 quad_perm:[1,0,3,2] row_mask:0xf bank_mask:0xf\n\t"
      "v_bitop3_b32 %[t0], %[t0], %[kx], %[u0] bitop3:0xbe\n\t"
      "v_bitop3_b32 %[t1], %[t1], %[kx], %[u1] bitop3:0xbe\n\t"
      "v_bitop3_b32 %[t2], %[t2], %[kx], %[u2] bitop3:0xbe\n\t"
      "v_bitop3_b32 %[t3], %[t3], %[kx], %[u3] bitop3:0xbe\n\t"
      "v_cmp_ne_u32 %[m0], 0, %[t0]\n\t"
      "v_cmp_ne_u32 %[m1], 0, %[t1]\n\t"
      "v_cmp_ne_u32 %[m2], 0, %[t2]\n\t"
      "v_cmp_ne_u32 %[m3], 0, %[t3]\n\t"
      "s_or_b64 %[x0], %[m0], %[m1]\n\t"
      "s_or_b64 %[x1], %[m2], %[m3]\n\t"
      "s_or_b64 %[x0], %[x0], %[x1]\n\t"
      "s_cbranch_scc0 2f\n\t"
      "s_sub_u32 %[cnt], %[cnt], 1\n\t"
      "s_cbranch_scc1 2f\n\t"
      "s_sleep 2\n\t"
      "s_mov_b64 exec, %[m0]\n\t"
      "buffer_load_dwordx4 v[128:131], %[vo], %[rs], 0 offen sc1\n\t"
      "s_mov_b64 exec, %[m1]\n\t"
      "buffer_load_dwordx4 v[132:135], %[vo], %[rs], 0 offen offset:1024 sc1\n\t"
      "s_mov_b64 exec, %[m2]\n\t"
      "buffer_load_dwordx4 v[136:139], %[vo], %[rs], 0 offen offset:2048 sc1\n\t"
      "s_mov_b64 exec, %[m3]\n\t"
      "buffer_load_dwordx4 v[140:143], %[vo], %[rs], 0 offen offset:3072 sc1\n\t"
      "s_mov_b64 exec, -1\n\t"
      "s_branch 1b\n\t"
      "2:\n\t"
      : "={v[128:131]}"(gr[0]), "={v[132:135]}"(gr[1]), "={v[136:139]}"(gr[2]), "={v[140:143]}"(gr[3]),
        [t0] "=&v"(tt[0]), [t1] "=&v"(tt[1]), [t2] "=&v"(tt[2]), [t3] "=&v"(tt[3]),
        [u0] "=&v"(tu[0]), [u1] "=&v"(tu[1]), [u2] "=&v"(tu[2]), [u3] "=&v"(tu[3]),
        [m0] "=&s"(mm[0]), [m1] "=&s"(mm[1]), [m2] "=&s"(mm[2]), [m3] "=&s"(mm[3]),
        [x0] "=&s"(mx0), [x1] "=&s"(mx1), [cnt] "+s"(cnt)
      : [vo] "v"(vo), [rs] "s"(rdesc), [kx] "s"(KX)
      : "memory", "scc");
  }
  if (cnt == 0xffffffffu) return;
  float gl[4];
#pragma unroll
  for (int i = 0; i < 4; ++i) gl[i] = __uint_as_float(gr[i][1] ^ hA);
  float M = fmaxf(fmaxf(gl[0], gl[1]), fmaxf(gl[2], gl[3]));
  M = xg_max(fmaxf(M, dpp<0x128>(M)));
  const float Ms = (M == -INFINITY) ? 0.f : M;
  float W = 0.f, a0 = 0.f, a1 = 0.f, a2 = 0.f, a3 = 0.f;
#pragma unroll
  for (int i = 0; i < 4; ++i) {
    const float e = __builtin_amdgcn_exp2f(gl[i] - Ms);
    W += e;
    a0 = fmaf(e, __uint_as_float(gr[i][0] << 16), a0); a1 = fmaf(e, __uint_as_float(gr[i][0] & 0xffff0000u), a1);
    a2 = fmaf(e, __uint_as_float(gr[i][2] << 16), a2); a3 = fmaf(e, __uint_as_float(gr[i][2] & 0xffff0000u), a3);
  }
  const auto s01 = __builtin_amdgcn_permlane32_swap(__float_as_uint(a0), __float_as_uint(a1), false, false);
  const auto s23 = __builtin_amdgcn_permlane32_swap(__float_as_uint(a2), __float_as_uint(a3), false, false);
  const float u01 = __uint_as_float(s01[0]) + __uint_as_float(s01[1]);
  const float u23 = __uint_as_float(s23[0]) + __uint_as_float(s23[1]);
  const auto sx = __builtin_amdgcn_permlane16_swap(__float_as_uint(u01), __float_as_uint(u23), false, false);
  float v = __uint_as_float(sx[0]) + __uint_as_float(sx[1]);
  v += dpp<0x128>(v);
  W = xg_sum(W + dpp<0x128>(W));
  float* slot = lds + LDS_FIN + w * FIN_ROW;
  unsigned tk = 0;
  if (n < 8) {
    slot[4 * n + 2 * (g & 1) + (g >> 1)] = v;
    if (lane == 0) {
      *(f32x2*)(slot + 32) = f32x2{M, W};
      CB();
      int ci = 5;
      asm volatile("" : "+v"(ci));
      tk = LDS_ADD(ctl + ci, 1u);
    }
  }
  CB();
  const float* fin = lds + LDS_FIN;
  const int dd = lane & 31;
  float mw[8], ww[8], av[8];
#pragma unroll
  for (int i = 0; i < 8; ++i) {
    const f32x2 x = *(const f32x2*)(fin + i * FIN_ROW + 32);
    mw[i] = x[0]; ww[i] = x[1]; av[i] = fin[i * FIN_ROW + dd];
  }
  CB();
  if (__builtin_amdgcn_readfirstlane(tk) != 7u) return;
  M = fmaxf(fmaxf(fmaxf(mw[0], mw[1]), fmaxf(mw[2], mw[3])), fmaxf(fmaxf(mw[4], mw[5]), fmaxf(mw[6], mw[7])));
  const float Mf = (M == -INFINITY) ? 0.f : M;
  float Wt = 0.f, at = 0.f;
#pragma unroll
  for (int i = 0; i < 8; ++i) {
    const float fi = __builtin_amdgcn_exp2f(mw[i] - Mf);
    Wt = fmaf(fi, ww[i], Wt); at = fmaf(fi, av[i], at);
  }
  if (__builtin_expect(ctx < LK && !(M > -1.0e9f * LOG2E + 2048.f), 0)) {
    const float lm = -1.0e9f * LOG2E;
    const float Mall = fmaxf(M, lm);
    const float wv = (M == -INFINITY) ? 0.f : __builtin_amdgcn_exp2f(M - Mall);
    const float wm = __builtin_amdgcn_exp2f(lm - Mall);
    float sv = 0.f;
    const int hk = h >> 3;
    for (int t = ctx + (lane >> 5); t < LK; t += 2) {
      const unsigned short d = *(const unsigned short*)(vc + ((size_t)bt[t / BS] * BS + (t % BS)) * (HKV * D * 2) + hk * (D * 2) + (u * 32 + dd) * 2);
      sv += __uint_as_float((unsigned)d << 16);
    }
    sv += __shfl_xor(sv, 32);
    at = at * wv + sv * wm;
    Wt = Wt * wv + wm * (float)(LK - ctx);
  }
  const float res = at * __builtin_amdgcn_rcpf(Wt);
  const unsigned pk = cvtpk(res, dpp<0xB1>(res));
  const __amdgpu_buffer_rsrc_t ro = __builtin_amdgcn_make_buffer_rsrc((void*)out, 0, H * D * 2, 0x00020000);
  __builtin_amdgcn_raw_buffer_store_b32(pk, ro, (lane < 32 && !(lane & 1)) ? (h * D + u * 32 + lane) * 2 : OOB, 0, AUX_SC1);
  if (c == 0 && lane == 0) __hip_atomic_store((unsigned*)ws, e1_, __ATOMIC_RELAXED, __HIP_MEMORY_SCOPE_AGENT);
}

}

extern "C" void kernel_launch(void* const* d_in, const int* in_sizes, int n_in, void* d_out, int out_size, void* d_ws,
                              size_t ws_size, hipStream_t stream) {
  attn_decode<<<NWG, NTHR, 0, stream>>>((const char*)d_in[0], (const char*)d_in[1], (const char*)d_in[2], (const int*)d_in[3],
                                        (const int*)d_in[4], (char*)d_ws, (char*)d_out, in_sizes[1] * 2, 0);
}
```

```cpp
#include <hip/hip_runtime.h>
#include <hip/hip_bf16.h>
#include <stdint.h>

namespace {

constexpr int H = 32, HKV = 4, D = 128, BS = 16, NB = 4096, G = H / HKV;
constexpr int LK = NB * BS;
constexpr int NWG = 256, NWAVE = 8, NTHR = NWAVE * 64;
constexpr float SCALE = 0.08838834764831845f;
constexpr float LOG2E = 1.4426950408889634f;
constexpr float C2 = SCALE * LOG2E;
constexpr int PSTEP = 2 * NWG * BS;
constexpr int OOB = 0x40000000;
constexpr int CODE_BYTES = 7000;
constexpr int WS_VAL = 4096;
constexpr int WS_BYTES = WS_VAL + H * 4 * NWG * 128;
constexpr int AUX_NT = 2, AUX_SC1 = 16;

typedef unsigned v4u __attribute__((ext_vector_type(4)));
typedef float f32x4 __attribute__((ext_vector_type(4)));
typedef __bf16 bf16x8 __attribute__((ext_vector_type(8)));
typedef __bf16 bf16x2 __attribute__((ext_vector_type(2)));
typedef float f32x2 __attribute__((ext_vector_type(2)));
#define LAS __attribute__((address_space(3)))
#define CAS __attribute__((address_space(4)))

__device__ __forceinline__ f32x4 mfma(v4u a, v4u b, f32x4 c) {
  return __builtin_amdgcn_mfma_f32_16x16x32_bf16(__builtin_bit_cast(bf16x8, a), __builtin_bit_cast(bf16x8, b), c, 0, 0, 0);
}
__device__ __forceinline__ unsigned cvtpk(float lo, float hi) {
  f32x2 v = {lo, hi};
  bf16x2 r = __builtin_convertvector(v, bf16x2);
  return __builtin_bit_cast(unsigned, r);
}
template <int CTRL> __device__ __forceinline__ float dpp(float x) {
  return __builtin_bit_cast(float, __builtin_amdgcn_mov_dpp(__builtin_bit_cast(int, x), CTRL, 0xf, 0xf, true));
}
__device__ __forceinline__ float xg_max(float x) {
  auto s = __builtin_amdgcn_permlane16_swap(__float_as_uint(x), __float_as_uint(x), false, false);
  x = fmaxf(__uint_as_float(s[0]), __uint_as_float(s[1]));
  auto t = __builtin_amdgcn_permlane32_swap(__float_as_uint(x), __float_as_uint(x), false, false);
  return fmaxf(__uint_as_float(t[0]), __uint_as_float(t[1]));
}
__device__ __forceinline__ float xg_sum(float x) {
  auto s = __builtin_amdgcn_permlane16_swap(__float_as_uint(x), __float_as_uint(x), false, false);
  x = __uint_as_float(s[0]) + __uint_as_float(s[1]);
  auto t = __builtin_amdgcn_permlane32_swap(__float_as_uint(x), __float_as_uint(x), false, false);
  return __uint_as_float(t[0]) + __uint_as_float(t[1]);
}
template <int R> __device__ __forceinline__ float rowb1(float x) {
  int t = __builtin_amdgcn_update_dpp(0, __builtin_bit_cast(int, x), 0x150 + R, 0x5, 0xf, false);
  t = __builtin_amdgcn_update_dpp(t, __builtin_bit_cast(int, x), 0x150 + 4 + R, 0xa, 0xf, false);
  return __builtin_bit_cast(float, t);
}
__device__ __forceinline__ f32x4 rowb(float x) { return f32x4{rowb1<0>(x), rowb1<1>(x), rowb1<2>(x), rowb1<3>(x)}; }

struct Page { v4u r[4]; };

__device__ __forceinline__ Page load_k(__amdgpu_buffer_rsrc_t rs, int so, int vo) {
  Page p;
  p.r[0] = __builtin_amdgcn_raw_buffer_load_b128(rs, vo, so, AUX_NT);
  p.r[1] = __builtin_amdgcn_raw_buffer_load_b128(rs, vo + 128, so, AUX_NT);
  p.r[2] = __builtin_amdgcn_raw_buffer_load_b128(rs, vo, so + 8192, AUX_NT);
  p.r[3] = __builtin_amdgcn_raw_buffer_load_b128(rs, vo + 128, so + 8192, AUX_NT);
  return p;
}
__device__ __forceinline__ Page load_v(__amdgpu_buffer_rsrc_t rs, int so, int vo) {
  Page p;
  p.r[0] = __builtin_amdgcn_raw_buffer_load_b128(rs, vo, so, AUX_NT);
  p.r[1] = __builtin_amdgcn_raw_buffer_load_b128(rs, vo + 1024, so, AUX_NT);
  p.r[2] = __builtin_amdgcn_raw_buffer_load_b128(rs, vo + 2048, so, AUX_NT);
  p.r[3] = __builtin_amdgcn_raw_buffer_load_b128(rs, vo + 3072, so, AUX_NT);
  return p;
}

struct QFrag { v4u q[2]; };
__device__ __forceinline__ f32x4 qk_page(const Page& k, const QFrag& q) {
  const f32x4 z = {0.f, 0.f, 0.f, 0.f};
  f32x4 d0 = mfma(k.r[0], q.q[0], z);
  f32x4 d1 = mfma(k.r[2], q.q[0], z);
  d0 = mfma(k.r[1], q.q[1], d0);
  d1 = mfma(k.r[3], q.q[1], d1);
  f32x4 x;
#pragma unroll
  for (int r = 0; r < 4; ++r) {
    auto s = __builtin_amdgcn_permlane32_swap(__float_as_uint(d0[r]), __float_as_uint(d1[r]), false, false);
    x[r] = __uint_as_float(s[0]) + dpp<0x128>(__uint_as_float(s[1]));
  }
  return x;
}

struct State { float m, l, lse; f32x4 acc[8]; };

template <bool LAST>
__device__ __forceinline__ v4u softmax_step(State& st, f32x4 xa, f32x4 xb, int rema, int g4, bool fin) {
  if (LAST && rema - PSTEP < BS) {
    const int lima = rema - g4, limb = lima - PSTEP;
#pragma unroll
    for (int r = 0; r < 4; ++r) {
      xa[r] = (r < lima) ? xa[r] : -INFINITY;
      xb[r] = (r < limb) ? xb[r] : -INFINITY;
    }
  }
  float mx = fmaxf(fmaxf(fmaxf(xa[0], xa[1]), fmaxf(xa[2], xa[3])), fmaxf(fmaxf(xb[0], xb[1]), fmaxf(xb[2], xb[3])));
  mx = xg_max(mx);
  const float mn = fmaxf(st.m, mx);
  const float mc = (LAST && mn == -INFINITY) ? 0.f : mn * C2;
  float pa[4], pb[4];
#pragma unroll
  for (int r = 0; r < 4; ++r) {
    pa[r] = __builtin_amdgcn_exp2f(fmaf(xa[r], C2, -mc));
    pb[r] = __builtin_amdgcn_exp2f(fmaf(xb[r], C2, -mc));
  }
  const float ps = ((pa[0] + pa[1]) + (pa[2] + pa[3])) + ((pb[0] + pb[1]) + (pb[2] + pb[3]));
  float alpha = __builtin_amdgcn_exp2f(fmaf(st.m, C2, -mc));
  st.l = fmaf(st.l, alpha, ps);
  if (LAST) {
    st.l = xg_sum(st.l);
    const float inv = fin ? (st.l > 0.f ? __builtin_amdgcn_rcpf(st.l) : 0.f) : 1.f;
    st.lse = mc + __builtin_amdgcn_logf(st.l);
    alpha *= inv;
#pragma unroll
    for (int r = 0; r < 4; ++r) { pa[r] *= inv; pb[r] *= inv; }
  }
  const f32x4 av = rowb(alpha);
#pragma unroll
  for (int m = 0; m < 8; ++m) st.acc[m] *= av;
  st.m = mn;
  v4u p;
  p[0] = cvtpk(pa[0], pa[1]);
  p[1] = cvtpk(pa[2], pa[3]);
  p[2] = cvtpk(pb[0], pb[1]);
  p[3] = cvtpk(pb[2], pb[3]);
  return p;
}

__device__ __forceinline__ void pv_step(State& st, const Page& va, const Page& vb, v4u p) {
#pragma unroll
  for (int m = 0; m < 8; ++m) {
    const int w = m >> 1;
    const unsigned sel = (m & 1) ? 0x07060302u : 0x05040100u;
    v4u f;
    f[0] = __builtin_amdgcn_perm(va.r[1][w], va.r[0][w], sel);
    f[1] = __builtin_amdgcn_perm(va.r[3][w], va.r[2][w], sel);
    f[2] = __builtin_amdgcn_perm(vb.r[1][w], vb.r[0][w], sel);
    f[3] = __builtin_amdgcn_perm(vb.r[3][w], vb.r[2][w], sel);
    st.acc[m] = mfma(p, f, st.acc[m]);
  }
}

constexpr int REC = 8 * 32 * 4 + 16;
constexpr int LDS_CTL = HKV * REC;
constexpr int LDS_FIN = LDS_CTL + 8;
constexpr int FIN_ROW = 36;
constexpr int LDS_WORDS = LDS_FIN + 8 * FIN_ROW;

__device__ __forceinline__ void merge_record(State& st, const float* rec, int lane, int g) {
  f32x4 o[8];
#pragma unroll
  for (int m = 0; m < 8; ++m) o[m] = *(const f32x4*)(rec + (m * 32 + (lane & 31)) * 4);
  const f32x2 ml = *(const f32x2*)(rec + 1024 + 2 * (lane & 7));
  const float M = fmaxf(st.m, ml[0]);
  const float Mc = (M == -INFINITY) ? 0.f : M * C2;
  const float e1 = __builtin_amdgcn_exp2f(fmaf(st.m, C2, -Mc)), e2 = __builtin_amdgcn_exp2f(fmaf(ml[0], C2, -Mc));
  st.l = fmaf(st.l, e1, g == 0 ? e2 * ml[1] : 0.f);
  st.m = M;
  const f32x4 v1 = rowb(e1), v2 = rowb(e2);
#pragma unroll
  for (int m = 0; m < 8; ++m) st.acc[m] = st.acc[m] * v1 + o[m] * v2;
}

#define SB() do { asm volatile("" ::: "memory"); __builtin_amdgcn_sched_barrier(0); } while (0)
#define CB() asm volatile("" ::: "memory")
#define LDS_ADD(p, v) __hip_atomic_fetch_add((LAS unsigned*)(p), (v), __ATOMIC_RELAXED, __HIP_MEMORY_SCOPE_WORKGROUP)

__global__ void __launch_bounds__(NTHR, 2)
attn_decode(const char* q, const char* kc, const char* vc, const int* bt, const int* ctx_lens,
            char* ws, char* out, long kv_bytes64) {
  __shared__ __attribute__((aligned(16))) float lds[LDS_WORDS];
  const int tid = threadIdx.x, lane = tid & 63;
  const __amdgpu_buffer_rsrc_t rc =
      __builtin_amdgcn_make_buffer_rsrc((void*)(__builtin_amdgcn_s_getpc() & ~127ull), 0, CODE_BYTES, 0x00020000);
  const unsigned cpf = __builtin_amdgcn_raw_buffer_load_b32(rc, tid * 128, 0, 0);
  SB();
  volatile LAS unsigned* ctl = (volatile LAS unsigned*)(lds + LDS_CTL);
  ctl[tid & 7] = 0u;
  const int w = __builtin_amdgcn_readfirstlane(tid >> 6);
  const int kvh = w & 3, sub = w >> 2, c = blockIdx.x;
  const int n = lane & 15, g = lane >> 4;
  asm volatile("s_waitcnt lgkmcnt(0)" :: "s"(q), "s"(kc), "s"(vc), "s"(bt), "s"(ctx_lens), "s"(ws), "s"(out), "s"(kv_bytes64) : "memory");
  const int kv_bytes = (int)kv_bytes64;

  const int pc = (c + 64 * kvh) & (NWG - 1);
  const __amdgpu_buffer_rsrc_t rb = __builtin_amdgcn_make_buffer_rsrc((void*)bt, 0, NB * 4, 0x00020000);
  const int physv = __builtin_amdgcn_raw_buffer_load_b32(rb, lane * (2 * NWG * 4), (sub * NWG + pc) * 4, 0);
  unsigned tch = 0;
  if (w == 7) {
    const char* tp = ((lane & 1) ? vc : kc) + (size_t)min((c >> 3) << 21, kv_bytes - 4);
    if (lane >= 2) tp = (c >> 3) < 3 ? ws + min(WS_VAL + ((c >> 3) << 21), WS_BYTES - 4) : out;
    if (lane <= 2) tch = __hip_atomic_load((const unsigned*)tp, __ATOMIC_RELAXED, __HIP_MEMORY_SCOPE_AGENT);
  }
  CB();
  int ctx = *(const CAS int*)ctx_lens;
  __builtin_amdgcn_s_barrier();
  if (sub == 0) __builtin_amdgcn_s_setprio(1);
  if (w == 4) {
    const unsigned e = *(const CAS unsigned*)ws + 1u;
    ctl[6] = e; ctl[7] = 1u;
  }

  QFrag qf;
  const char* qrow = q + (kvh * G + (n & 7)) * (D * 2) + (n >> 3) * 64 + g * 16;
  qf.q[0] = *(const v4u*)(qrow);
  qf.q[1] = *(const v4u*)(qrow + 128);
  const int koff = (n & 7) * 1024 + (n >> 3) * 64 + g * 16;
  const int voff = g * 4096 + n * 16;
  const __amdgpu_buffer_rsrc_t rk = __builtin_amdgcn_make_buffer_rsrc((void*)(kc + kvh * 256), 0, kv_bytes - kvh * 256, 0x00020000);
  const __amdgpu_buffer_rsrc_t rv = __builtin_amdgcn_make_buffer_rsrc((void*)(vc + kvh * 256), 0, kv_bytes - kvh * 256, 0x00020000);
  State st;
  st.m = -INFINITY; st.l = 0.f; st.lse = -INFINITY;
#pragma unroll
  for (int m = 0; m < 8; ++m) st.acc[m] = f32x4{0.f, 0.f, 0.f, 0.f};
  float* rec = lds + kvh * REC;

  SB();
  ctx = ctx < 0 ? 0 : (ctx > LK ? LK : ctx);
  int rem = ctx - (sub * NWG + pc) * BS;
  const int sov = (rem - lane * PSTEP > 0) ? (physv & 0xffff) << 14 : OOB;

  int s0 = __builtin_amdgcn_readlane(sov, 0), s1 = __builtin_amdgcn_readlane(sov, 1);
  Page KA = load_k(rk, s0, koff), KB = load_k(rk, s1, koff);
  Page VA = load_v(rv, s0, voff), VB = load_v(rv, s1, voff);
  SB();
  while (__builtin_amdgcn_readfirstlane(ctl[7]) == 0u) __builtin_amdgcn_s_sleep(1);
  const unsigned e1_ = __builtin_amdgcn_readfirstlane(ctl[6]);
  SB();
#pragma clang loop unroll(disable)
  for (int i = 2; rem - 2 * PSTEP > 0; i += 2, rem -= 2 * PSTEP) {
    s0 = __builtin_amdgcn_readlane(sov, i); s1 = __builtin_amdgcn_readlane(sov, i + 1);
    const f32x4 xa = qk_page(KA, qf), xb = qk_page(KB, qf);
    SB();
    KA = load_k(rk, s0, koff); KB = load_k(rk, s1, koff);
    SB();
    const v4u p = softmax_step<false>(st, xa, xb, 0, 0, false);
    SB();
    pv_step(st, VA, VB, p);
    SB();
    VA = load_v(rv, s0, voff); VB = load_v(rv, s1, voff);
    SB();
  }
  if (sub) {
    while (__builtin_amdgcn_readfirstlane(ctl[kvh]) == 0u) __builtin_amdgcn_s_sleep(1);
    CB();
    merge_record(st, rec, lane, g);
  }
  SB();
  const unsigned hA = e1_ * 0x9E3779B1u, KX = (e1_ << 1) | 1u, hB = hA ^ KX;
  v4u ta, tb;
  int pvo = WS_VAL + ((((kvh * G + 4 * (g & 1)) * 4 + (n >> 2)) * NWG + c) * 8 + 2 * (n & 3) + (g >> 1)) * 16;
  {
    const f32x4 xa = qk_page(KA, qf), xb = qk_page(KB, qf);
    const v4u p = softmax_step<true>(st, xa, xb, rem, 4 * g, sub != 0);
    const f32x4 lb = rowb(st.lse);
#pragma unroll
    for (int r = 0; r < 4; ++r) { ta[r] = __float_as_uint(lb[r]) ^ hA; tb[r] = __float_as_uint(lb[r]) ^ hB; }
    asm volatile("" : "+v"(ta), "+v"(tb), "+v"(pvo));
    SB();
    pv_step(st, VA, VB, p);
  }
  asm volatile("" :: "v"(cpf), "v"(tch));
  if (sub) {
    const __amdgpu_buffer_rsrc_t rw = __builtin_amdgcn_make_buffer_rsrc((void*)ws, 0, WS_BYTES, 0x00020000);
#pragma unroll
    for (int r = 0; r < 4; ++r) {
      const unsigned w0 = cvtpk(st.acc[0][r], st.acc[1][r]), w1 = cvtpk(st.acc[2][r], st.acc[3][r]);
      const unsigned w2 = cvtpk(st.acc[4][r], st.acc[5][r]), w3 = cvtpk(st.acc[6][r], st.acc[7][r]);
      const auto x0 = __builtin_amdgcn_permlane32_swap(w0, w2, false, false);
      const auto x1 = __builtin_amdgcn_permlane32_swap(w1, w3, false, false);
      const v4u gv = {x0[0], ta[r], x1[0], tb[r]};
      __builtin_amdgcn_raw_buffer_store_b128(gv, rw, pvo + r * (4 * NWG * 128), 0, AUX_SC1);
    }
    if (lane == 0) LDS_ADD(ctl + 4, 1u);
  } else {
    if (lane < 32) {
#pragma unroll
      for (int m = 0; m < 8; ++m) *(f32x4*)(rec + (m * 32 + lane) * 4) = st.acc[m];
      if (lane < G) *(f32x2*)(rec + 1024 + 2 * lane) = f32x2{st.m, st.l};
    }
    CB();
    if (lane == 0) ctl[kvh] = 1u;
    __builtin_amdgcn_s_setprio(0);
  }
  if (c & 1) return;

  const int h = c >> 3, u = (c >> 1) & 3;
  const int vo = WS_VAL + ((c >> 1) * NWG + 32 * w) * 128 + lane * 16;
  const v4u rdesc = {(unsigned)(uintptr_t)ws, (unsigned)((uintptr_t)ws >> 32) & 0xffffu, (unsigned)WS_BYTES, 0x00020000u};
  while (__builtin_amdgcn_readfirstlane(ctl[4]) < 4u) __builtin_amdgcn_s_sleep(1);
  __builtin_amdgcn_s_sleep(10);
  unsigned cnt = 1u << 20;
  v4u gr[4];
  {
    unsigned tt[4], tu[4];
    unsigned long long mm[4], mx0, mx1;
    asm volatile(
      "s_nop 4\n\t"
      "buffer_load_dwordx4 v[128:131], %[vo], %[rs], 0 offen sc1\n\t"
      "buffer_load_dwordx4 v[132:135], %[vo], %[rs], 0 offen offset:1024 sc1\n\t"
      "buffer_load_dwordx4 v[136:139], %[vo], %[rs], 0 offen offset:2048 sc1\n\t"
      "buffer_load_dwordx4 v[140:143], %[vo], %[rs], 0 offen offset:3072 sc1\n\t"
      "1:\n\t"
      "s_waitcnt vmcnt(0)\n\t"
      "v_xor_b32 %[t0], v129, v131\n\t"
      "v_xor_b32 %[t1], v133, v135\n\t"
      "v_xor_b32 %[t2], v137, v139\n\t"
      "v_xor_b32 %[t3], v141, v143\n\t"
      "v_xor_b32_dpp %[u0], v129, v129 quad_perm:[1,0,3,2] row_mask:0xf bank_mask:0xf\n\t"
      "v_xor_b32_dpp %[u1], v133, v133 quad_perm:[1,0,3,2] row_mask:0xf bank_mask:0xf\n\t"
      "v_xor_b32_dpp %[u2], v137, v137 quad_perm:[1,0,3,2] row_mask:0xf bank_mask:0xf\n\t"
      "v_xor_b32_dpp %[u3], v141, v141 quad_perm:[1,0,3,2] row_mask:0xf bank_mask:0xf\n\t"
      "v_bitop3_b32 %[t0], %[t0], %[kx], %[u0] bitop3:0xbe\n\t"
      "v_bitop3_b32 %[t1], %[t1], %[kx], %[u1] bitop3:0xbe\n\t"
      "v_bitop3_b32 %[t2], %[t2], %[kx], %[u2] bitop3:0xbe\n\t"
      "v_bitop3_b32 %[t3], %[t3], %[kx], %[u3] bitop3:0xbe\n\t"
      "v_cmp_ne_u32 %[m0], 0, %[t0]\n\t"
      "v_cmp_ne_u32 %[m1], 0, %[t1]\n\t"
      "v_cmp_ne_u32 %[m2], 0, %[t2]\n\t"
      "v_cmp_ne_u32 %[m3], 0, %[t3]\n\t"
      "s_or_b64 %[x0], %[m0], %[m1]\n\t"
      "s_or_b64 %[x1], %[m2], %[m3]\n\t"
      "s_or_b64 %[x0], %[x0], %[x1]\n\t"
      "s_cbranch_scc0 2f\n\t"
      "s_sub_u32 %[cnt], %[cnt], 1\n\t"
      "s_cbranch_scc1 2f\n\t"
      "s_sleep 2\n\t"
      "s_mov_b64 exec, %[m0]\n\t"
      "s_cbranch_execz 3f\n\t"
      "buffer_load_dwordx4 v[128:131], %[vo], %[rs], 0 offen sc1\n\t"
      "3:\n\t"
      "s_mov_b64 exec, %[m1]\n\t"
      "s_cbranch_execz 4f\n\t"
      "buffer_load_dwordx4 v[132:135], %[vo], %[rs], 0 offen offset:1024 sc1\n\t"
      "4:\n\t"
      "s_mov_b64 exec, %[m2]\n\t"
      "s_cbranch_execz 5f\n\t"
      "buffer_load_dwordx4 v[136:139], %[vo], %[rs], 0 offen offset:2048 sc1\n\t"
      "5:\n\t"
      "s_mov_b64 exec, %[m3]\n\t"
      "s_cbranch_execz 6f\n\t"
      "buffer_load_dwordx4 v[140:143], %[vo], %[rs], 0 offen offset:3072 sc1\n\t"
      "6:\n\t"
      "s_mov_b64 exec, -1\n\t"
      "s_branch 1b\n\t"
      "2:\n\t"
      : "=&{v[128:131]}"(gr[0]), "=&{v[132:135]}"(gr[1]), "=&{v[136:139]}"(gr[2]), "=&{v[140:143]}"(gr[3]),
        [t0] "=&v"(tt[0]), [t1] "=&v"(tt[1]), [t2] "=&v"(tt[2]), [t3] "=&v"(tt[3]),
        [u0] "=&v"(tu[0]), [u1] "=&v"(tu[1]), [u2] "=&v"(tu[2]), [u3] "=&v"(tu[3]),
        [m0] "=&s"(mm[0]), [m1] "=&s"(mm[1]), [m2] "=&s"(mm[2]), [m3] "=&s"(mm[3]),
        [x0] "=&s"(mx0), [x1] "=&s"(mx1), [cnt] "+s"(cnt)
      : [vo] "v"(vo), [rs] "s"(rdesc), [kx] "s"(KX)
      : "memory", "scc");
  }
  if (cnt == 0xffffffffu) return;
  float gl[4];
#pragma unroll
  for (int i = 0; i < 4; ++i) gl[i] = __uint_as_float(gr[i][1] ^ hA);
  float M = fmaxf(fmaxf(gl[0], gl[1]), fmaxf(gl[2], gl[3]));
  M = xg_max(fmaxf(M, dpp<0x128>(M)));
  const float Ms = (M == -INFINITY) ? 0.f : M;
  float W = 0.f, a0 = 0.f, a1 = 0.f, a2 = 0.f, a3 = 0.f;
#pragma unroll
  for (int i = 0; i < 4; ++i) {
    const float e = __builtin_amdgcn_exp2f(gl[i] - Ms);
    W += e;
    a0 = fmaf(e, __uint_as_float(gr[i][0] << 16), a0); a1 = fmaf(e, __uint_as_float(gr[i][0] & 0xffff0000u), a1);
    a2 = fmaf(e, __uint_as_float(gr[i][2] << 16), a2); a3 = fmaf(e, __uint_as_float(gr[i][2] & 0xffff0000u), a3);
  }
  const auto s01 = __builtin_amdgcn_permlane32_swap(__float_as_uint(a0), __float_as_uint(a1), false, false);
  const auto s23 = __builtin_amdgcn_permlane32_swap(__float_as_uint(a2), __float_as_uint(a3), false, false);
  const float u01 = __uint_as_float(s01[0]) + __uint_as_float(s01[1]);
  const float u23 = __uint_as_float(s23[0]) + __uint_as_float(s23[1]);
  const auto sx = __builtin_amdgcn_permlane16_swap(__float_as_uint(u01), __float_as_uint(u23), false, false);
  float v = __uint_as_float(sx[0]) + __uint_as_float(sx[1]);
  v += dpp<0x128>(v);
  W = xg_sum(W + dpp<0x128>(W));
  float* slot = lds + LDS_FIN + w * FIN_ROW;
  unsigned tk = 0;
  if (n < 8) {
    slot[4 * n + 2 * (g & 1) + (g >> 1)] = v;
    if (lane == 0) {
      *(f32x2*)(slot + 32) = f32x2{M, W};
      CB();
      int ci = 5;
      asm volatile("" : "+v"(ci));
      tk = LDS_ADD(ctl + ci, 1u);
    }
  }
  CB();
  const float* fin = lds + LDS_FIN;
  const int dd = lane & 31;
  float mw[8], ww[8], av[8];
#pragma unroll
  for (int i = 0; i < 8; ++i) {
    const f32x2 x = *(const f32x2*)(fin + i * FIN_ROW + 32);
    mw[i] = x[0]; ww[i] = x[1]; av[i] = fin[i * FIN_ROW + dd];
  }
  CB();
  if (__builtin_amdgcn_readfirstlane(tk) != 7u) return;
  M = fmaxf(fmaxf(fmaxf(mw[0], mw[1]), fmaxf(mw[2], mw[3])), fmaxf(fmaxf(mw[4], mw[5]), fmaxf(mw[6], mw[7])));
  const float Mf = (M == -INFINITY) ? 0.f : M;
  float Wt = 0.f, at = 0.f;
#pragma unroll
  for (int i = 0; i < 8; ++i) {
    const float fi = __builtin_amdgcn_exp2f(mw[i] - Mf);
    Wt = fmaf(fi, ww[i], Wt); at = fmaf(fi, av[i], at);
  }
  if (__builtin_expect(ctx < LK && !(M > -1.0e9f * LOG2E + 2048.f), 0)) {
    const float lm = -1.0e9f * LOG2E;
    const float Mall = fmaxf(M, lm);
    const float wv = (M == -INFINITY) ? 0.f : __builtin_amdgcn_exp2f(M - Mall);
    const float wm = __builtin_amdgcn_exp2f(lm - Mall);
    float sv = 0.f;
    const int hk = h >> 3;
    for (int t = ctx + (lane >> 5); t < LK; t += 2) {
      const unsigned short d = *(const unsigned short*)(vc + ((size_t)bt[t / BS] * BS + (t % BS)) * (HKV * D * 2) + hk * (D * 2) + (u * 32 + dd) * 2);
      sv += __uint_as_float((unsigned)d << 16);
    }
    sv += __shfl_xor(sv, 32);
    at = at * wv + sv * wm;
    Wt = Wt * wv + wm * (float)(LK - ctx);
  }
  const float res = at * __builtin_amdgcn_rcpf(Wt);
  const unsigned pk = cvtpk(res, dpp<0xB1>(res));
  const __amdgpu_buffer_rsrc_t ro = __builtin_amdgcn_make_buffer_rsrc((void*)out, 0, H * D * 2, 0x00020000);
  __builtin_amdgcn_raw_buffer_store_b32(pk, ro, (lane < 32 && !(lane & 1)) ? (h * D + u * 32 + lane) * 2 : OOB, 0, AUX_SC1);
  if (c == 0 && lane == 0) __hip_atomic_store((unsigned*)ws, e1_, __ATOMIC_RELAXED, __HIP_MEMORY_SCOPE_AGENT);
}

}

extern "C" void kernel_launch(void* const* d_in, const int* in_sizes, int n_in, void* d_out, int out_size, void* d_ws,
                              size_t ws_size, hipStream_t stream) {
  attn_decode<<<NWG, NTHR, 0, stream>>>((const char*)d_in[0], (const char*)d_in[1], (const char*)d_in[2], (const int*)d_in[3],
                                        (const int*)d_in[4], (char*)d_ws, (char*)d_out, (long)in_sizes[1] * 2);
}
```

```cpp
#include <hip/hip_runtime.h>
#include <hip/hip_bf16.h>
#include <stdint.h>

namespace {

constexpr int H = 32, HKV = 4, D = 128, BS = 16, NB = 4096, G = H / HKV;
constexpr int LK = NB * BS;
constexpr int NWG = 256, NWAVE = 8, NTHR = NWAVE * 64;
constexpr float SCALE = 0.08838834764831845f;
constexpr float LOG2E = 1.4426950408889634f;
constexpr float C2 = SCALE * LOG2E;
constexpr int PSTEP = 2 * NWG * BS;
constexpr int OOB = 0x40000000;
constexpr int CODE_BYTES = 7000;
constexpr int WS_VAL = 4096;
constexpr int WS_BYTES = WS_VAL + H * 4 * NWG * 128;
constexpr int AUX_NT = 2, AUX_SC1 = 16;

typedef unsigned v4u __attribute__((ext_vector_type(4)));
typedef float f32x4 __attribute__((ext_vector_type(4)));
typedef __bf16 bf16x8 __attribute__((ext_vector_type(8)));
typedef __bf16 bf16x2 __attribute__((ext_vector_type(2)));
typedef float f32x2 __attribute__((ext_vector_type(2)));
#define LAS __attribute__((address_space(3)))
#define CAS __attribute__((address_space(4)))

__device__ __forceinline__ f32x4 mfma(v4u a, v4u b, f32x4 c) {
  return __builtin_amdgcn_mfma_f32_16x16x32_bf16(__builtin_bit_cast(bf16x8, a), __builtin_bit_cast(bf16x8, b), c, 0, 0, 0);
}
__device__ __forceinline__ unsigned cvtpk(float lo, float hi) {
  f32x2 v = {lo, hi};
  bf16x2 r = __builtin_convertvector(v, bf16x2);
  return __builtin_bit_cast(unsigned, r);
}
template <int CTRL> __device__ __forceinline__ float dpp(float x) {
  return __builtin_bit_cast(float, __builtin_amdgcn_mov_dpp(__builtin_bit_cast(int, x), CTRL, 0xf, 0xf, true));
}
__device__ __forceinline__ float xg_max(float x) {
  auto s = __builtin_amdgcn_permlane16_swap(__float_as_uint(x), __float_as_uint(x), false, false);
  x = fmaxf(__uint_as_float(s[0]), __uint_as_float(s[1]));
  auto t = __builtin_amdgcn_permlane32_swap(__float_as_uint(x), __float_as_uint(x), false, false);
  return fmaxf(__uint_as_float(t[0]), __uint_as_float(t[1]));
}
__device__ __forceinline__ float xg_sum(float x) {
  auto s = __builtin_amdgcn_permlane16_swap(__float_as_uint(x), __float_as_uint(x), false, false);
  x = __uint_as_float(s[0]) + __uint_as_float(s[1]);
  auto t = __builtin_amdgcn_permlane32_swap(__float_as_uint(x), __float_as_uint(x), false, false);
  return __uint_as_float(t[0]) + __uint_as_float(t[1]);
}
template <int R> __device__ __forceinline__ float rowb1(float x) {
  int t = __builtin_amdgcn_update_dpp(0, __builtin_bit_cast(int, x), 0x150 + R, 0x5, 0xf, false);
  t = __builtin_amdgcn_update_dpp(t, __builtin_bit_cast(int, x), 0x150 + 4 + R, 0xa, 0xf, false);
  return __builtin_bit_cast(float, t);
}
__device__ __forceinline__ f32x4 rowb(float x) { return f32x4{rowb1<0>(x), rowb1<1>(x), rowb1<2>(x), rowb1<3>(x)}; }

struct Page { v4u r[4]; };

__device__ __forceinline__ Page load_k(__amdgpu_buffer_rsrc_t rs, int so, int vo) {
  Page p;
  p.r[0] = __builtin_amdgcn_raw_buffer_load_b128(rs, vo, so, AUX_NT);
  p.r[1] = __builtin_amdgcn_raw_buffer_load_b128(rs, vo + 128, so, AUX_NT);
  p.r[2] = __builtin_amdgcn_raw_buffer_load_b128(rs, vo, so + 8192, AUX_NT);
  p.r[3] = __builtin_amdgcn_raw_buffer_load_b128(rs, vo + 128, so + 8192, AUX_NT);
  return p;
}
__device__ __forceinline__ Page load_v(__amdgpu_buffer_rsrc_t rs, int so, int vo) {
  Page p;
  p.r[0] = __builtin_amdgcn_raw_buffer_load_b128(rs, vo, so, AUX_NT);
  p.r[1] = __builtin_amdgcn_raw_buffer_load_b128(rs, vo + 1024, so, AUX_NT);
  p.r[2] = __builtin_amdgcn_raw_buffer_load_b128(rs, vo + 2048, so, AUX_NT);
  p.r[3] = __builtin_amdgcn_raw_buffer_load_b128(rs, vo + 3072, so, AUX_NT);
  return p;
}

struct QFrag { v4u q[2]; };
__device__ __forceinline__ f32x4 qk_page(const Page& k, const QFrag& q) {
  const f32x4 z = {0.f, 0.f, 0.f, 0.f};
  f32x4 d0 = mfma(k.r[0], q.q[0], z);
  f32x4 d1 = mfma(k.r[2], q.q[0], z);
  d0 = mfma(k.r[1], q.q[1], d0);
  d1 = mfma(k.r[3], q.q[1], d1);
  f32x4 x;
#pragma unroll
  for (int r = 0; r < 4; ++r) {
    auto s = __builtin_amdgcn_permlane32_swap(__float_as_uint(d0[r]), __float_as_uint(d1[r]), false, false);
    x[r] = __uint_as_float(s[0]) + dpp<0x128>(__uint_as_float(s[1]));
  }
  return x;
}

struct State { float m, l, lse; f32x4 acc[8]; };

template <bool LAST>
__device__ __forceinline__ v4u softmax_step(State& st, f32x4 xa, f32x4 xb, int rema, int g4, bool fin) {
  if (LAST && rema - PSTEP < BS) {
    const int lima = rema - g4, limb = lima - PSTEP;
#pragma unroll
    for (int r = 0; r < 4; ++r) {
      xa[r] = (r < lima) ? xa[r] : -INFINITY;
      xb[r] = (r < limb) ? xb[r] : -INFINITY;
    }
  }
  float mx = fmaxf(fmaxf(fmaxf(xa[0], xa[1]), fmaxf(xa[2], xa[3])), fmaxf(fmaxf(xb[0], xb[1]), fmaxf(xb[2], xb[3])));
  mx = xg_max(mx);
  const float mn = fmaxf(st.m, mx);
  const float mc = (LAST && mn == -INFINITY) ? 0.f : mn * C2;
  float pa[4], pb[4];
#pragma unroll
  for (int r = 0; r < 4; ++r) {
    pa[r] = __builtin_amdgcn_exp2f(fmaf(xa[r], C2, -mc));
    pb[r] = __builtin_amdgcn_exp2f(fmaf(xb[r], C2, -mc));
  }
  const float ps = ((pa[0] + pa[1]) + (pa[2] + pa[3])) + ((pb[0] + pb[1]) + (pb[2] + pb[3]));
  float alpha = __builtin_amdgcn_exp2f(fmaf(st.m, C2, -mc));
  st.l = fmaf(st.l, alpha, ps);
  if (LAST) {
    st.l = xg_sum(st.l);
    const float inv = fin ? (st.l > 0.f ? __builtin_amdgcn_rcpf(st.l) : 0.f) : 1.f;
    st.lse = mc + __builtin_amdgcn_logf(st.l);
    alpha *= inv;
#pragma unroll
    for (int r = 0; r < 4; ++r) { pa[r] *= inv; pb[r] *= inv; }
  }
  const f32x4 av = rowb(alpha);
#pragma unroll
  for (int m = 0; m < 8; ++m) st.acc[m] *= av;
  st.m = mn;
  v4u p;
  p[0] = cvtpk(pa[0], pa[1]);
  p[1] = cvtpk(pa[2], pa[3]);
  p[2] = cvtpk(pb[0], pb[1]);
  p[3] = cvtpk(pb[2], pb[3]);
  return p;
}

__device__ __forceinline__ void pv_step(State& st, const Page& va, const Page& vb, v4u p) {
#pragma unroll
  for (int m = 0; m < 8; ++m) {
    const int w = m >> 1;
    const unsigned sel = (m & 1) ? 0x07060302u : 0x05040100u;
    v4u f;
    f[0] = __builtin_amdgcn_perm(va.r[1][w], va.r[0][w], sel);
    f[1] = __builtin_amdgcn_perm(va.r[3][w], va.r[2][w], sel);
    f[2] = __builtin_amdgcn_perm(vb.r[1][w], vb.r[0][w], sel);
    f[3] = __builtin_amdgcn_perm(vb.r[3][w], vb.r[2][w], sel);
    st.acc[m] = mfma(p, f, st.acc[m]);
  }
}

constexpr int REC = 8 * 32 * 4 + 16;
constexpr int LDS_CTL = HKV * REC;
constexpr int LDS_FIN = LDS_CTL + 8;
constexpr int FIN_ROW = 36;
constexpr int LDS_WORDS = LDS_FIN + 8 * FIN_ROW;

__device__ __forceinline__ void merge_record(State& st, const float* rec, int lane, int g) {
  f32x4 o[8];
#pragma unroll
  for (int m = 0; m < 8; ++m) o[m] = *(const f32x4*)(rec + (m * 32 + (lane & 31)) * 4);
  const f32x2 ml = *(const f32x2*)(rec + 1024 + 2 * (lane & 7));
  const float M = fmaxf(st.m, ml[0]);
  const float Mc = (M == -INFINITY) ? 0.f : M * C2;
  const float e1 = __builtin_amdgcn_exp2f(fmaf(st.m, C2, -Mc)), e2 = __builtin_amdgcn_exp2f(fmaf(ml[0], C2, -Mc));
  st.l = fmaf(st.l, e1, g == 0 ? e2 * ml[1] : 0.f);
  st.m = M;
  const f32x4 v1 = rowb(e1), v2 = rowb(e2);
#pragma unroll
  for (int m = 0; m < 8; ++m) st.acc[m] = st.acc[m] * v1 + o[m] * v2;
}

#define SB() do { asm volatile("" ::: "memory"); __builtin_amdgcn_sched_barrier(0); } while (0)
#define CB() asm volatile("" ::: "memory")
#define LDS_ADD(p, v) __hip_atomic_fetch_add((LAS unsigned*)(p), (v), __ATOMIC_RELAXED, __HIP_MEMORY_SCOPE_WORKGROUP)

__global__ void __launch_bounds__(NTHR, 2)
attn_decode(const char* q, const char* kc, const char* vc, const int* bt, const int* ctx_lens,
            char* ws, char* out, int kv_bytes, int pad_) {
  __shared__ __attribute__((aligned(16))) float lds[LDS_WORDS];
  const int tid = threadIdx.x, lane = tid & 63;
  const __amdgpu_buffer_rsrc_t rc =
      __builtin_amdgcn_make_buffer_rsrc((void*)(__builtin_amdgcn_s_getpc() & ~127ull), 0, CODE_BYTES, 0x00020000);
  const unsigned cpf = __builtin_amdgcn_raw_buffer_load_b32(rc, tid * 128, 0, 0);
  SB();
  volatile LAS unsigned* ctl = (volatile LAS unsigned*)(lds + LDS_CTL);
  ctl[tid & 7] = 0u;
  const int w = __builtin_amdgcn_readfirstlane(tid >> 6);
  const int kvh = w & 3, sub = w >> 2, c = blockIdx.x;
  const int n = lane & 15, g = lane >> 4;
  asm volatile("s_waitcnt lgkmcnt(0)" :: "s"(q), "s"(kc), "s"(vc), "s"(bt), "s"(ctx_lens), "s"(ws), "s"(out), "s"(kv_bytes) : "memory");

  const int pc = (c + 64 * kvh) & (NWG - 1);
  const __amdgpu_buffer_rsrc_t rb = __builtin_amdgcn_make_buffer_rsrc((void*)bt, 0, NB * 4, 0x00020000);
  const int physv = __builtin_amdgcn_raw_buffer_load_b32(rb, lane * (2 * NWG * 4), (sub * NWG + pc) * 4, 0);
  unsigned tch = 0;
  if (w == 7) {
    const char* tp = ((lane & 1) ? vc : kc) + (size_t)min((c >> 3) << 21, kv_bytes - 4);
    if (lane >= 2) tp = (c >> 3) < 3 ? ws + min(WS_VAL + ((c >> 3) << 21), WS_BYTES - 4) : out;
    if (lane <= 2) tch = __hip_atomic_load((const unsigned*)tp, __ATOMIC_RELAXED, __HIP_MEMORY_SCOPE_AGENT);
  }
  CB();
  int ctx = *(const CAS int*)ctx_lens;
  __builtin_amdgcn_s_barrier();
  if (sub == 0) __builtin_amdgcn_s_setprio(1);
  if (w == 4) {
    const unsigned e = *(const CAS unsigned*)ws + 1u;
    ctl[6] = e; ctl[7] = 1u;
  }

  QFrag qf;
  const char* qrow = q + (kvh * G + (n & 7)) * (D * 2) + (n >> 3) * 64 + g * 16;
  qf.q[0] = *(const v4u*)(qrow);
  qf.q[1] = *(const v4u*)(qrow + 128);
  const int koff = (n & 7) * 1024 + (n >> 3) * 64 + g * 16;
  const int voff = g * 4096 + n * 16;
  const __amdgpu_buffer_rsrc_t rk = __builtin_amdgcn_make_buffer_rsrc((void*)(kc + kvh * 256), 0, kv_bytes - kvh * 256, 0x00020000);
  const __amdgpu_buffer_rsrc_t rv = __builtin_amdgcn_make_buffer_rsrc((void*)(vc + kvh * 256), 0, kv_bytes - kvh * 256, 0x00020000);
  State st;
  st.m = -INFINITY; st.l = 0.f; st.lse = -INFINITY;
#pragma unroll
  for (int m = 0; m < 8; ++m) st.acc[m] = f32x4{0.f, 0.f, 0.f, 0.f};
  float* rec = lds + kvh * REC;

  SB();
  ctx = ctx < 0 ? 0 : (ctx > LK ? LK : ctx);
  int rem = ctx - (sub * NWG + pc) * BS;
  const int sov = (rem - lane * PSTEP > 0) ? (physv & 0xffff) << 14 : OOB;

  int s0 = __builtin_amdgcn_readlane(sov, 0), s1 = __builtin_amdgcn_readlane(sov, 1);
  Page KA = load_k(rk, s0, koff), KB = load_k(rk, s1, koff);
  Page VA = load_v(rv, s0, voff), VB = load_v(rv, s1, voff);
  SB();
  while (__builtin_amdgcn_readfirstlane(ctl[7]) == 0u) __builtin_amdgcn_s_sleep(1);
  const unsigned e1_ = __builtin_amdgcn_readfirstlane(ctl[6]);
  SB();
#pragma clang loop unroll(disable)
  for (int i = 2; rem - 2 * PSTEP > 0; i += 2, rem -= 2 * PSTEP) {
    s0 = __builtin_amdgcn_readlane(sov, i); s1 = __builtin_amdgcn_readlane(sov, i + 1);
    const f32x4 xa = qk_page(KA, qf), xb = qk_page(KB, qf);
    SB();
    KA = load_k(rk, s0, koff); KB = load_k(rk, s1, koff);
    SB();
    const v4u p = softmax_step<false>(st, xa, xb, 0, 0, false);
    SB();
    pv_step(st, VA, VB, p);
    SB();
    VA = load_v(rv, s0, voff); VB = load_v(rv, s1, voff);
    SB();
  }
  if (sub) {
    while (__builtin_amdgcn_readfirstlane(ctl[kvh]) == 0u) __builtin_amdgcn_s_sleep(1);
    CB();
    merge_record(st, rec, lane, g);
  }
  SB();
  const unsigned hA = e1_ * 0x9E3779B1u, KX = (e1_ << 1) | 1u, hB = hA ^ KX;
  v4u ta, tb;
  int pvo = WS_VAL + ((((kvh * G + 4 * (g & 1)) * 4 + (n >> 2)) * NWG + c) * 8 + 2 * (n & 3) + (g >> 1)) * 16;
  {
    const f32x4 xa = qk_page(KA, qf), xb = qk_page(KB, qf);
    const v4u p = softmax_step<true>(st, xa, xb, rem, 4 * g, sub != 0);
    const f32x4 lb = rowb(st.lse);
#pragma unroll
    for (int r = 0; r < 4; ++r) { ta[r] = __float_as_uint(lb[r]) ^ hA; tb[r] = __float_as_uint(lb[r]) ^ hB; }
    asm volatile("" : "+v"(ta), "+v"(tb), "+v"(pvo));
    SB();
    pv_step(st, VA, VB, p);
  }
  asm volatile("" :: "v"(cpf), "v"(tch));
  if (sub) {
    const __amdgpu_buffer_rsrc_t rw = __builtin_amdgcn_make_buffer_rsrc((void*)ws, 0, WS_BYTES, 0x00020000);
#pragma unroll
    for (int r = 0; r < 4; ++r) {
      const unsigned w0 = cvtpk(st.acc[0][r], st.acc[1][r]), w1 = cvtpk(st.acc[2][r], st.acc[3][r]);
      const unsigned w2 = cvtpk(st.acc[4][r], st.acc[5][r]), w3 = cvtpk(st.acc[6][r], st.acc[7][r]);
      const auto x0 = __builtin_amdgcn_permlane32_swap(w0, w2, false, false);
      const auto x1 = __builtin_amdgcn_permlane32_swap(w1, w3, false, false);
      const v4u gv = {x0[0], ta[r], x1[0], tb[r]};
      __builtin_amdgcn_raw_buffer_store_b128(gv, rw, pvo + r * (4 * NWG * 128), 0, AUX_SC1);
    }
    if (lane == 0) LDS_ADD(ctl + 4, 1u);
  } else {
    if (lane < 32) {
#pragma unroll
      for (int m = 0; m < 8; ++m) *(f32x4*)(rec + (m * 32 + lane) * 4) = st.acc[m];
      if (lane < G) *(f32x2*)(rec + 1024 + 2 * lane) = f32x2{st.m, st.l};
    }
    CB();
    if (lane == 0) ctl[kvh] = 1u;
    __builtin_amdgcn_s_setprio(0);
  }
  if (c & 1) return;

  const int h = c >> 3, u = (c >> 1) & 3;
  const int vo = WS_VAL + ((c >> 1) * NWG + 32 * w) * 128 + lane * 16;
  const v4u rdesc = {(unsigned)(uintptr_t)ws, (unsigned)((uintptr_t)ws >> 32) & 0xffffu, (unsigned)WS_BYTES, 0x00020000u};
  while (__builtin_amdgcn_readfirstlane(ctl[4]) < 4u) __builtin_amdgcn_s_sleep(1);
  __builtin_amdgcn_s_sleep(10);
  unsigned cnt = 1u << 20;
  v4u gr[4];
  {
    unsigned tt[4], tu[4];
    unsigned long long mm[4], mx0, mx1;
    asm volatile(
      "s_nop 4\n\t"
      "buffer_load_dwordx4 v[128:131], %[vo], %[rs], 0 offen sc1\n\t"
      "buffer_load_dwordx4 v[132:135], %[vo], %[rs], 0 offen offset:1024 sc1\n\t"
      "buffer_load_dwordx4 v[136:139], %[vo], %[rs], 0 offen offset:2048 sc1\n\t"
      "buffer_load_dwordx4 v[140:143], %[vo], %[rs], 0 offen offset:3072 sc1\n\t"
      "1:\n\t"
      "s_waitcnt vmcnt(0)\n\t"
      "v_xor_b32 %[t0], v129, v131\n\t"
      "v_xor_b32 %[t1], v133, v135\n\t"
      "v_xor_b32 %[t2], v137, v139\n\t"
      "v_xor_b32 %[t3], v141, v143\n\t"
      "v_xor_b32_dpp %[u0], v129, v129 quad_perm:[1,0,3,2] row_mask:0xf bank_mask:0xf\n\t"
      "v_xor_b32_dpp %[u1], v133, v133 quad_perm:[1,0,3,2] row_mask:0xf bank_mask:0xf\n\t"
      "v_xor_b32_dpp %[u2], v137, v137 quad_perm:[1,0,3,2] row_mask:0xf bank_mask:0xf\n\t"
      "v_xor_b32_dpp %[u3], v141, v141 quad_perm:[1,0,3,2] row_mask:0xf bank_mask:0xf\n\t"
      "v_bitop3_b32 %[t0], %[t0], %[kx], %[u0] bitop3:0xbe\n\t"
      "v_bitop3_b32 %[t1], %[t1], %[kx], %[u1] bitop3:0xbe\n\t"
      "v_bitop3_b32 %[t2], %[t2], %[kx], %[u2] bitop3:0xbe\n\t"
      "v_bitop3_b32 %[t3], %[t3], %[kx], %[u3] bitop3:0xbe\n\t"
      "v_cmp_ne_u32 %[m0], 0, %[t0]\n\t"
      "v_cmp_ne_u32 %[m1], 0, %[t1]\n\t"
      "v_cmp_ne_u32 %[m2], 0, %[t2]\n\t"
      "v_cmp_ne_u32 %[m3], 0, %[t3]\n\t"
      "s_or_b64 %[x0], %[m0], %[m1]\n\t"
      "s_or_b64 %[x1], %[m2], %[m3]\n\t"
      "s_or_b64 %[x0], %[x0], %[x1]\n\t"
      "s_cbranch_scc0 2f\n\t"
      "s_sub_u32 %[cnt], %[cnt], 1\n\t"
      "s_cbranch_scc1 2f\n\t"
      "s_sleep 2\n\t"
      "s_mov_b64 exec, %[m0]\n\t"
      "s_cbranch_execz 3f\n\t"
      "buffer_load_dwordx4 v[128:131], %[vo], %[rs], 0 offen sc1\n\t"
      "3:\n\t"
      "s_mov_b64 exec, %[m1]\n\t"
      "s_cbranch_execz 4f\n\t"
      "buffer_load_dwordx4 v[132:135], %[vo], %[rs], 0 offen offset:1024 sc1\n\t"
      "4:\n\t"
      "s_mov_b64 exec, %[m2]\n\t"
      "s_cbranch_execz 5f\n\t"
      "buffer_load_dwordx4 v[136:139], %[vo], %[rs], 0 offen offset:2048 sc1\n\t"
      "5:\n\t"
      "s_mov_b64 exec, %[m3]\n\t"
      "s_cbranch_execz 6f\n\t"
      "buffer_load_dwordx4 v[140:143], %[vo], %[rs], 0 offen offset:3072 sc1\n\t"
      "6:\n\t"
      "s_mov_b64 exec, -1\n\t"
      "s_branch 1b\n\t"
      "2:\n\t"
      : "=&{v[128:131]}"(gr[0]), "=&{v[132:135]}"(gr[1]), "=&{v[136:139]}"(gr[2]), "=&{v[140:143]}"(gr[3]),
        [t0] "=&v"(tt[0]), [t1] "=&v"(tt[1]), [t2] "=&v"(tt[2]), [t3] "=&v"(tt[3]),
        [u0] "=&v"(tu[0]), [u1] "=&v"(tu[1]), [u2] "=&v"(tu[2]), [u3] "=&v"(tu[3]),
        [m0] "=&s"(mm[0]), [m1] "=&s"(mm[1]), [m2] "=&s"(mm[2]), [m3] "=&s"(mm[3]),
        [x0] "=&s"(mx0), [x1] "=&s"(mx1), [cnt] "+s"(cnt)
      : [vo] "v"(vo), [rs] "s"(rdesc), [kx] "s"(KX)
      : "memory", "scc");
  }
  if (cnt == 0xffffffffu) return;
  float gl[4];
#pragma unroll
  for (int i = 0; i < 4; ++i) gl[i] = __uint_as_float(gr[i][1] ^ hA);
  float M = fmaxf(fmaxf(gl[0], gl[1]), fmaxf(gl[2], gl[3]));
  M = xg_max(fmaxf(M, dpp<0x128>(M)));
  const float Ms = (M == -INFINITY) ? 0.f : M;
  float W = 0.f, a0 = 0.f, a1 = 0.f, a2 = 0.f, a3 = 0.f;
#pragma unroll
  for (int i = 0; i < 4; ++i) {
    const float e = __builtin_amdgcn_exp2f(gl[i] - Ms);
    W += e;
    a0 = fmaf(e, __uint_as_float(gr[i][0] << 16), a0); a1 = fmaf(e, __uint_as_float(gr[i][0] & 0xffff0000u), a1);
    a2 = fmaf(e, __uint_as_float(gr[i][2] << 16), a2); a3 = fmaf(e, __uint_as_float(gr[i][2] & 0xffff0000u), a3);
  }
  const auto s01 = __builtin_amdgcn_permlane32_swap(__float_as_uint(a0), __float_as_uint(a1), false, false);
  const auto s23 = __builtin_amdgcn_permlane32_swap(__float_as_uint(a2), __float_as_uint(a3), false, false);
  const float u01 = __uint_as_float(s01[0]) + __uint_as_float(s01[1]);
  const float u23 = __uint_as_float(s23[0]) + __uint_as_float(s23[1]);
  const auto sx = __builtin_amdgcn_permlane16_swap(__float_as_uint(u01), __float_as_uint(u23), false, false);
  float v = __uint_as_float(sx[0]) + __uint_as_float(sx[1]);
  v += dpp<0x128>(v);
  W = xg_sum(W + dpp<0x128>(W));
  float* slot = lds + LDS_FIN + w * FIN_ROW;
  unsigned tk = 0;
  if (n < 8) {
    slot[4 * n + 2 * (g & 1) + (g >> 1)] = v;
    if (lane == 0) {
      *(f32x2*)(slot + 32) = f32x2{M, W};
      CB();
      int ci = 5;
      asm volatile("" : "+v"(ci));
      tk = LDS_ADD(ctl + ci, 1u);
    }
  }
  CB();
  const float* fin = lds + LDS_FIN;
  const int dd = lane & 31;
  float mw[8], ww[8], av[8];
#pragma unroll
  for (int i = 0; i < 8; ++i) {
    const f32x2 x = *(const f32x2*)(fin + i * FIN_ROW + 32);
    mw[i] = x[0]; ww[i] = x[1]; av[i] = fin[i * FIN_ROW + dd];
  }
  CB();
  if (__builtin_amdgcn_readfirstlane(tk) != 7u) return;
  M = fmaxf(fmaxf(fmaxf(mw[0], mw[1]), fmaxf(mw[2], mw[3])), fmaxf(fmaxf(mw[4], mw[5]), fmaxf(mw[6], mw[7])));
  const float Mf = (M == -INFINITY) ? 0.f : M;
  float Wt = 0.f, at = 0.f;
#pragma unroll
  for (int i = 0; i < 8; ++i) {
    const float fi = __builtin_amdgcn_exp2f(mw[i] - Mf);
    Wt = fmaf(fi, ww[i], Wt); at = fmaf(fi, av[i], at);
  }
  if (__builtin_expect(ctx < LK && !(M > -1.0e9f * LOG2E + 2048.f), 0)) {
    const float lm = -1.0e9f * LOG2E;
    const float Mall = fmaxf(M, lm);
    const float wv = (M == -INFINITY) ? 0.f : __builtin_amdgcn_exp2f(M - Mall);
    const float wm = __builtin_amdgcn_exp2f(lm - Mall);
    float sv = 0.f;
    const int hk = h >> 3;
    for (int t = ctx + (lane >> 5); t < LK; t += 2) {
      const unsigned short d = *(const unsigned short*)(vc + ((size_t)bt[t / BS] * BS + (t % BS)) * (HKV * D * 2) + hk * (D * 2) + (u * 32 + dd) * 2);
      sv += __uint_as_float((unsigned)d << 16);
    }
    sv += __shfl_xor(sv, 32);
    at = at * wv + sv * wm;
    Wt = Wt * wv + wm * (float)(LK - ctx);
  }
  const float res = at * __builtin_amdgcn_rcpf(Wt);
  const unsigned pk = cvtpk(res, dpp<0xB1>(res));
  const __amdgpu_buffer_rsrc_t ro = __builtin_amdgcn_make_buffer_rsrc((void*)out, 0, H * D * 2, 0x00020000);
  __builtin_amdgcn_raw_buffer_store_b32(pk, ro, (lane < 32 && !(lane & 1)) ? (h * D + u * 32 + lane) * 2 : OOB, 0, AUX_SC1);
  if (c == 0 && lane == 0) __hip_atomic_store((unsigned*)ws, e1_, __ATOMIC_RELAXED, __HIP_MEMORY_SCOPE_AGENT);
}

}

extern "C" void kernel_launch(void* const* d_in, const int* in_sizes, int n_in, void* d_out, int out_size, void* d_ws,
                              size_t ws_size, hipStream_t stream) {
  attn_decode<<<NWG, NTHR, 0, stream>>>((const char*)d_in[0], (const char*)d_in[1], (const char*)d_in[2], (const int*)d_in[3],
                                        (const int*)d_in[4], (char*)d_ws, (char*)d_out, in_sizes[1] * 2, 0);
}
```

```cpp
#include <hip/hip_runtime.h>
#include <hip/hip_bf16.h>
#include <stdint.h>

namespace {

constexpr int H = 32, HKV = 4, D = 128, BS = 16, NB = 4096, G = H / HKV;
constexpr int LK = NB * BS;
constexpr int NWG = 256, NWAVE = 8, NTHR = NWAVE * 64;
constexpr float SCALE = 0.08838834764831845f;
constexpr float LOG2E = 1.4426950408889634f;
constexpr float C2 = SCALE * LOG2E;
constexpr int PSTEP = 2 * NWG * BS;
constexpr int OOB = 0x40000000;
constexpr int CODE_BYTES = 6900;
constexpr int WS_VAL = 4096;
constexpr int WS_BYTES = WS_VAL + H * 4 * NWG * 128;
constexpr int AUX_NT = 2, AUX_SC1 = 16;

typedef unsigned v4u __attribute__((ext_vector_type(4)));
typedef float f32x4 __attribute__((ext_vector_type(4)));
typedef __bf16 bf16x8 __attribute__((ext_vector_type(8)));
typedef __bf16 bf16x2 __attribute__((ext_vector_type(2)));
typedef float f32x2 __attribute__((ext_vector_type(2)));
#define LAS __attribute__((address_space(3)))
#define CAS __attribute__((address_space(4)))

__device__ __forceinline__ f32x4 mfma(v4u a, v4u b, f32x4 c) {
  return __builtin_amdgcn_mfma_f32_16x16x32_bf16(__builtin_bit_cast(bf16x8, a), __builtin_bit_cast(bf16x8, b), c, 0, 0, 0);
}
__device__ __forceinline__ unsigned cvtpk(float lo, float hi) {
  f32x2 v = {lo, hi};
  bf16x2 r = __builtin_convertvector(v, bf16x2);
  return __builtin_bit_cast(unsigned, r);
}
template <int CTRL> __device__ __forceinline__ float dpp(float x) {
  return __builtin_bit_cast(float, __builtin_amdgcn_mov_dpp(__builtin_bit_cast(int, x), CTRL, 0xf, 0xf, true));
}
__device__ __forceinline__ float xg_max(float x) {
  auto s = __builtin_amdgcn_permlane16_swap(__float_as_uint(x), __float_as_uint(x), false, false);
  x = fmaxf(__uint_as_float(s[0]), __uint_as_float(s[1]));
  auto t = __builtin_amdgcn_permlane32_swap(__float_as_uint(x), __float_as_uint(x), false, false);
  return fmaxf(__uint_as_float(t[0]), __uint_as_float(t[1]));
}
__device__ __forceinline__ float xg_sum(float x) {
  auto s = __builtin_amdgcn_permlane16_swap(__float_as_uint(x), __float_as_uint(x), false, false);
  x = __uint_as_float(s[0]) + __uint_as_float(s[1]);
  auto t = __builtin_amdgcn_permlane32_swap(__float_as_uint(x), __float_as_uint(x), false, false);
  return __uint_as_float(t[0]) + __uint_as_float(t[1]);
}
template <int R> __device__ __forceinline__ float rowb1(float x) {
  int t = __builtin_amdgcn_update_dpp(0, __builtin_bit_cast(int, x), 0x150 + R, 0x5, 0xf, false);
  t = __builtin_amdgcn_update_dpp(t, __builtin_bit_cast(int, x), 0x150 + 4 + R, 0xa, 0xf, false);
  return __builtin_bit_cast(float, t);
}
__device__ __forceinline__ f32x4 rowb(float x) { return f32x4{rowb1<0>(x), rowb1<1>(x), rowb1<2>(x), rowb1<3>(x)}; }

struct Page { v4u r[4]; };

__device__ __forceinline__ Page load_k(__amdgpu_buffer_rsrc_t rs, int so, int vo) {
  Page p;
  p.r[0] = __builtin_amdgcn_raw_buffer_load_b128(rs, vo, so, AUX_NT);
  p.r[1] = __builtin_amdgcn_raw_buffer_load_b128(rs, vo + 128, so, AUX_NT);
  p.r[2] = __builtin_amdgcn_raw_buffer_load_b128(rs, vo, so + 8192, AUX_NT);
  p.r[3] = __builtin_amdgcn_raw_buffer_load_b128(rs, vo + 128, so + 8192, AUX_NT);
  return p;
}
__device__ __forceinline__ Page load_v(__amdgpu_buffer_rsrc_t rs, int so, int vo) {
  Page p;
  p.r[0] = __builtin_amdgcn_raw_buffer_load_b128(rs, vo, so, AUX_NT);
  p.r[1] = __builtin_amdgcn_raw_buffer_load_b128(rs, vo + 1024, so, AUX_NT);
  p.r[2] = __builtin_amdgcn_raw_buffer_load_b128(rs, vo + 2048, so, AUX_NT);
  p.r[3] = __builtin_amdgcn_raw_buffer_load_b128(rs, vo + 3072, so, AUX_NT);
  return p;
}

struct QFrag { v4u q[2]; };
__device__ __forceinline__ f32x4 qk_page(const Page& k, const QFrag& q) {
  const f32x4 z = {0.f, 0.f, 0.f, 0.f};
  f32x4 d0 = mfma(k.r[0], q.q[0], z);
  f32x4 d1 = mfma(k.r[2], q.q[0], z);
  d0 = mfma(k.r[1], q.q[1], d0);
  d1 = mfma(k.r[3], q.q[1], d1);
  f32x4 x;
#pragma unroll
  for (int r = 0; r < 4; ++r) {
    auto s = __builtin_amdgcn_permlane32_swap(__float_as_uint(d0[r]), __float_as_uint(d1[r]), false, false);
    x[r] = __uint_as_float(s[0]) + dpp<0x128>(__uint_as_float(s[1]));
  }
  return x;
}

struct State { float m, l, lse; f32x4 acc[8]; };

template <bool LAST>
__device__ __forceinline__ v4u softmax_step(State& st, f32x4 xa, f32x4 xb, int rema, int g4, bool fin) {
  if (LAST && rema - PSTEP < BS) {
    const int lima = rema - g4, limb = lima - PSTEP;
#pragma unroll
    for (int r = 0; r < 4; ++r) {
      xa[r] = (r < lima) ? xa[r] : -INFINITY;
      xb[r] = (r < limb) ? xb[r] : -INFINITY;
    }
  }
  float mx = fmaxf(fmaxf(fmaxf(xa[0], xa[1]), fmaxf(xa[2], xa[3])), fmaxf(fmaxf(xb[0], xb[1]), fmaxf(xb[2], xb[3])));
  mx = xg_max(mx);
  const float mn = fmaxf(st.m, mx);
  const float mc = (LAST && mn == -INFINITY) ? 0.f : mn * C2;
  float pa[4], pb[4];
#pragma unroll
  for (int r = 0; r < 4; ++r) {
    pa[r] = __builtin_amdgcn_exp2f(fmaf(xa[r], C2, -mc));
    pb[r] = __builtin_amdgcn_exp2f(fmaf(xb[r], C2, -mc));
  }
  const float ps = ((pa[0] + pa[1]) + (pa[2] + pa[3])) + ((pb[0] + pb[1]) + (pb[2] + pb[3]));
  float alpha = __builtin_amdgcn_exp2f(fmaf(st.m, C2, -mc));
  st.l = fmaf(st.l, alpha, ps);
  if (LAST) {
    st.l = xg_sum(st.l);
    const float inv = fin ? (st.l > 0.f ? __builtin_amdgcn_rcpf(st.l) : 0.f) : 1.f;
    st.lse = mc + __builtin_amdgcn_logf(st.l);
    alpha *= inv;
#pragma unroll
    for (int r = 0; r < 4; ++r) { pa[r] *= inv; pb[r] *= inv; }
  }
  const f32x4 av = rowb(alpha);
#pragma unroll
  for (int m = 0; m < 8; ++m) st.acc[m] *= av;
  st.m = mn;
  v4u p;
  p[0] = cvtpk(pa[0], pa[1]);
  p[1] = cvtpk(pa[2], pa[3]);
  p[2] = cvtpk(pb[0], pb[1]);
  p[3] = cvtpk(pb[2], pb[3]);
  return p;
}

__device__ __forceinline__ void pv_step(State& st, const Page& va, const Page& vb, v4u p) {
#pragma unroll
  for (int m = 0; m < 8; ++m) {
    const int w = m >> 1;
    const unsigned sel = (m & 1) ? 0x07060302u : 0x05040100u;
    v4u f;
    f[0] = __builtin_amdgcn_perm(va.r[1][w], va.r[0][w], sel);
    f[1] = __builtin_amdgcn_perm(va.r[3][w], va.r[2][w], sel);
    f[2] = __builtin_amdgcn_perm(vb.r[1][w], vb.r[0][w], sel);
    f[3] = __builtin_amdgcn_perm(vb.r[3][w], vb.r[2][w], sel);
    st.acc[m] = mfma(p, f, st.acc[m]);
  }
}

constexpr int REC = 8 * 32 * 4 + 16;
constexpr int LDS_CTL = HKV * REC;
constexpr int LDS_FIN = LDS_CTL + 8;
constexpr int FIN_ROW = 36;
constexpr int LDS_WORDS = LDS_FIN + 8 * FIN_ROW;

__device__ __forceinline__ void merge_record(State& st, const float* rec, int lane, int g) {
  f32x4 o[8];
#pragma unroll
  for (int m = 0; m < 8; ++m) o[m] = *(const f32x4*)(rec + (m * 32 + (lane & 31)) * 4);
  const f32x2 ml = *(const f32x2*)(rec + 1024 + 2 * (lane & 7));
  const float M = fmaxf(st.m, ml[0]);
  const float Mc = (M == -INFINITY) ? 0.f : M * C2;
  const float e1 = __builtin_amdgcn_exp2f(fmaf(st.m, C2, -Mc)), e2 = __builtin_amdgcn_exp2f(fmaf(ml[0], C2, -Mc));
  st.l = fmaf(st.l, e1, g == 0 ? e2 * ml[1] : 0.f);
  st.m = M;
  const f32x4 v1 = rowb(e1), v2 = rowb(e2);
#pragma unroll
  for (int m = 0; m < 8; ++m) st.acc[m] = st.acc[m] * v1 + o[m] * v2;
}

#define SB() do { asm volatile("" ::: "memory"); __builtin_amdgcn_sched_barrier(0); } while (0)
#define CB() asm volatile("" ::: "memory")
#define LDS_ADD(p, v) __hip_atomic_fetch_add((LAS unsigned*)(p), (v), __ATOMIC_RELAXED, __HIP_MEMORY_SCOPE_WORKGROUP)

__global__ void __launch_bounds__(NTHR, 2)
attn_decode(const char* q, const char* kc, const char* vc, const int* bt, const int* ctx_lens,
            char* ws, char* out, int kv_bytes, int pad_) {
  __shared__ __attribute__((aligned(16))) float lds[LDS_WORDS];
  const int tid = threadIdx.x, lane = tid & 63;
  const __amdgpu_buffer_rsrc_t rc =
      __builtin_amdgcn_make_buffer_rsrc((void*)(__builtin_amdgcn_s_getpc() & ~127ull), 0, CODE_BYTES, 0x00020000);
  const unsigned cpf = __builtin_amdgcn_raw_buffer_load_b32(rc, tid * 128, 0, 0);
  SB();
  const int w = __builtin_amdgcn_readfirstlane(tid >> 6);
  const int kvh = w & 3, sub = w >> 2, c = blockIdx.x;
  const int n = lane & 15, g = lane >> 4;
  asm volatile("" :: "s"(q), "s"(kc), "s"(vc), "s"(bt), "s"(ctx_lens), "s"(ws), "s"(out), "s"(kv_bytes));

  const int pc = (c + 64 * kvh) & (NWG - 1);
  const __amdgpu_buffer_rsrc_t rb = __builtin_amdgcn_make_buffer_rsrc((void*)bt, 0, NB * 4, 0x00020000);
  const int physv = __builtin_amdgcn_raw_buffer_load_b32(rb, lane * (2 * NWG * 4), (sub * NWG + pc) * 4, 0);
  unsigned tch = 0;
  if (w == 7) {
    const char* tp = ((lane & 1) ? vc : kc) + (size_t)min((c >> 3) << 21, kv_bytes - 4);
    if (lane >= 2) tp = (c >> 3) < 3 ? ws + min(WS_VAL + ((c >> 3) << 21), WS_BYTES - 4) : out;
    if (lane <= 2) tch = __hip_atomic_load((const unsigned*)tp, __ATOMIC_RELAXED, __HIP_MEMORY_SCOPE_AGENT);
  }
  CB();
  int ctx = *(const CAS int*)ctx_lens;
  const unsigned ep = *(const CAS unsigned*)ws;
  volatile LAS unsigned* ctl = (volatile LAS unsigned*)(lds + LDS_CTL);
  if (tid < 8) ctl[tid] = 0u;
  if (sub == 0) __builtin_amdgcn_s_setprio(1);

  QFrag qf;
  const char* qrow = q + (kvh * G + (n & 7)) * (D * 2) + (n >> 3) * 64 + g * 16;
  qf.q[0] = *(const v4u*)(qrow);
  qf.q[1] = *(const v4u*)(qrow + 128);
  const int koff = (n & 7) * 1024 + (n >> 3) * 64 + g * 16;
  const int voff = g * 4096 + n * 16;
  const __amdgpu_buffer_rsrc_t rk = __builtin_amdgcn_make_buffer_rsrc((void*)(kc + kvh * 256), 0, kv_bytes - kvh * 256, 0x00020000);
  const __amdgpu_buffer_rsrc_t rv = __builtin_amdgcn_make_buffer_rsrc((void*)(vc + kvh * 256), 0, kv_bytes - kvh * 256, 0x00020000);
  State st;
  st.m = -INFINITY; st.l = 0.f; st.lse = -INFINITY;
#pragma unroll
  for (int m = 0; m < 8; ++m) st.acc[m] = f32x4{0.f, 0.f, 0.f, 0.f};
  float* rec = lds + kvh * REC;

  asm volatile("s_waitcnt lgkmcnt(0)" :: "s"(ctx), "s"(ep) : "memory");
  __builtin_amdgcn_s_barrier();
  SB();
  ctx = ctx < 0 ? 0 : (ctx > LK ? LK : ctx);
  int rem = ctx - (sub * NWG + pc) * BS;
  const int sov = (rem - lane * PSTEP > 0) ? (physv & 0xffff) << 14 : OOB;

  int s0 = __builtin_amdgcn_readlane(sov, 0), s1 = __builtin_amdgcn_readlane(sov, 1);
  Page KA = load_k(rk, s0, koff), KB = load_k(rk, s1, koff);
  Page VA = load_v(rv, s0, voff), VB = load_v(rv, s1, voff);
  SB();
#pragma clang loop unroll(disable)
  for (int i = 2; rem - 2 * PSTEP > 0; i += 2, rem -= 2 * PSTEP) {
    s0 = __builtin_amdgcn_readlane(sov, i); s1 = __builtin_amdgcn_readlane(sov, i + 1);
    const f32x4 xa = qk_page(KA, qf), xb = qk_page(KB, qf);
    SB();
    KA = load_k(rk, s0, koff); KB = load_k(rk, s1, koff);
    SB();
    const v4u p = softmax_step<false>(st, xa, xb, 0, 0, false);
    SB();
    pv_step(st, VA, VB, p);
    SB();
    VA = load_v(rv, s0, voff); VB = load_v(rv, s1, voff);
    SB();
  }
  if (sub) {
    while (__builtin_amdgcn_readfirstlane(ctl[kvh]) == 0u) __builtin_amdgcn_s_sleep(1);
    CB();
    merge_record(st, rec, lane, g);
  }
  SB();
  const unsigned e1_ = ep + 1u, hA = e1_ * 0x9E3779B1u, KX = (e1_ << 1) | 1u, hB = hA ^ KX;
  v4u ta, tb;
  int pvo = WS_VAL + ((((kvh * G + 4 * (g & 1)) * 4 + (n >> 2)) * NWG + c) * 8 + 2 * (n & 3) + (g >> 1)) * 16;
  {
    const f32x4 xa = qk_page(KA, qf), xb = qk_page(KB, qf);
    const v4u p = softmax_step<true>(st, xa, xb, rem, 4 * g, sub != 0);
    const f32x4 lb = rowb(st.lse);
#pragma unroll
    for (int r = 0; r < 4; ++r) { ta[r] = __float_as_uint(lb[r]) ^ hA; tb[r] = __float_as_uint(lb[r]) ^ hB; }
    asm volatile("" : "+v"(ta), "+v"(tb), "+v"(pvo));
    SB();
    pv_step(st, VA, VB, p);
  }
  asm volatile("" :: "v"(cpf), "v"(tch));
  if (sub) {
    const __amdgpu_buffer_rsrc_t rw = __builtin_amdgcn_make_buffer_rsrc((void*)ws, 0, WS_BYTES, 0x00020000);
#pragma unroll
    for (int r = 0; r < 4; ++r) {
      const unsigned w0 = cvtpk(st.acc[0][r], st.acc[1][r]), w1 = cvtpk(st.acc[2][r], st.acc[3][r]);
      const unsigned w2 = cvtpk(st.acc[4][r], st.acc[5][r]), w3 = cvtpk(st.acc[6][r], st.acc[7][r]);
      const auto x0 = __builtin_amdgcn_permlane32_swap(w0, w2, false, false);
      const auto x1 = __builtin_amdgcn_permlane32_swap(w1, w3, false, false);
      const v4u gv = {x0[0], ta[r], x1[0], tb[r]};
      __builtin_amdgcn_raw_buffer_store_b128(gv, rw, pvo + r * (4 * NWG * 128), 0, AUX_SC1);
    }
    if (lane == 0) LDS_ADD(ctl + 4, 1u);
  } else {
    if (lane < 32) {
#pragma unroll
      for (int m = 0; m < 8; ++m) *(f32x4*)(rec + (m * 32 + lane) * 4) = st.acc[m];
      if (lane < G) *(f32x2*)(rec + 1024 + 2 * lane) = f32x2{st.m, st.l};
    }
    CB();
    if (lane == 0) ctl[kvh] = 1u;
    __builtin_amdgcn_s_setprio(0);
  }
  if (c & 1) return;

  const int h = c >> 3, u = (c >> 1) & 3;
  const int vo = WS_VAL + ((c >> 1) * NWG + 32 * w) * 128 + lane * 16;
  const v4u rdesc = {(unsigned)(uintptr_t)ws, (unsigned)((uintptr_t)ws >> 32) & 0xffffu, (unsigned)WS_BYTES, 0x00020000u};
  while (__builtin_amdgcn_readfirstlane(ctl[4]) < 4u) __builtin_amdgcn_s_sleep(1);
  __builtin_amdgcn_s_sleep(10);
  unsigned cnt = 1u << 20;
  v4u gr[4];
  {
    unsigned tt[4], tu[4];
    unsigned long long mm[4], mx0, mx1;
    asm volatile(
      "s_nop 4\n\t"
      "buffer_load_dwordx4 v[128:131], %[vo], %[rs], 0 offen sc1\n\t"
      "buffer_load_dwordx4 v[132:135], %[vo], %[rs], 0 offen offset:1024 sc1\n\t"
      "buffer_load_dwordx4 v[136:139], %[vo], %[rs], 0 offen offset:2048 sc1\n\t"
      "buffer_load_dwordx4 v[140:143], %[vo], %[rs], 0 offen offset:3072 sc1\n\t"
      "1:\n\t"
      "s_waitcnt vmcnt(0)\n\t"
      "v_xor_b32 %[t0], v129, v131\n\t"
      "v_xor_b32 %[t1], v133, v135\n\t"
      "v_xor_b32 %[t2], v137, v139\n\t"
      "v_xor_b32 %[t3], v141, v143\n\t"
      "v_xor_b32_dpp %[u0], v129, v129 quad_perm:[1,0,3,2] row_mask:0xf bank_mask:0xf\n\t"
      "v_xor_b32_dpp %[u1], v133, v133 quad_perm:[1,0,3,2] row_mask:0xf bank_mask:0xf\n\t"
      "v_xor_b32_dpp %[u2], v137, v137 quad_perm:[1,0,3,2] row_mask:0xf bank_mask:0xf\n\t"
      "v_xor_b32_dpp %[u3], v141, v141 quad_perm:[1,0,3,2] row_mask:0xf bank_mask:0xf\n\t"
      "v_bitop3_b32 %[t0], %[t0], %[kx], %[u0] bitop3:0xbe\n\t"
      "v_bitop3_b32 %[t1], %[t1], %[kx], %[u1] bitop3:0xbe\n\t"
      "v_bitop3_b32 %[t2], %[t2], %[kx], %[u2] bitop3:0xbe\n\t"
      "v_bitop3_b32 %[t3], %[t3], %[kx], %[u3] bitop3:0xbe\n\t"
      "v_cmp_ne_u32 %[m0], 0, %[t0]\n\t"
      "v_cmp_ne_u32 %[m1], 0, %[t1]\n\t"
      "v_cmp_ne_u32 %[m2], 0, %[t2]\n\t"
      "v_cmp_ne_u32 %[m3], 0, %[t3]\n\t"
      "s_or_b64 %[x0], %[m0], %[m1]\n\t"
      "s_or_b64 %[x1], %[m2], %[m3]\n\t"
      "s_or_b64 %[x0], %[x0], %[x1]\n\t"
      "s_cbranch_scc0 2f\n\t"
      "s_sub_u32 %[cnt], %[cnt], 1\n\t"
      "s_cbranch_scc1 2f\n\t"
      "s_sleep 2\n\t"
      "s_mov_b64 exec, %[m0]\n\t"
      "s_cbranch_execz 3f\n\t"
      "buffer_load_dwordx4 v[128:131], %[vo], %[rs], 0 offen sc1\n\t"
      "3:\n\t"
      "s_mov_b64 exec, %[m1]\n\t"
      "s_cbranch_execz 4f\n\t"
      "buffer_load_dwordx4 v[132:135], %[vo], %[rs], 0 offen offset:1024 sc1\n\t"
      "4:\n\t"
      "s_mov_b64 exec, %[m2]\n\t"
      "s_cbranch_execz 5f\n\t"
      "buffer_load_dwordx4 v[136:139], %[vo], %[rs], 0 offen offset:2048 sc1\n\t"
      "5:\n\t"
      "s_mov_b64 exec, %[m3]\n\t"
      "s_cbranch_execz 6f\n\t"
      "buffer_load_dwordx4 v[140:143], %[vo], %[rs], 0 offen offset:3072 sc1\n\t"
      "6:\n\t"
      "s_mov_b64 exec, -1\n\t"
      "s_branch 1b\n\t"
      "2:\n\t"
      : "=&{v[128:131]}"(gr[0]), "=&{v[132:135]}"(gr[1]), "=&{v[136:139]}"(gr[2]), "=&{v[140:143]}"(gr[3]),
        [t0] "=&v"(tt[0]), [t1] "=&v"(tt[1]), [t2] "=&v"(tt[2]), [t3] "=&v"(tt[3]),
        [u0] "=&v"(tu[0]), [u1] "=&v"(tu[1]), [u2] "=&v"(tu[2]), [u3] "=&v"(tu[3]),
        [m0] "=&s"(mm[0]), [m1] "=&s"(mm[1]), [m2] "=&s"(mm[2]), [m3] "=&s"(mm[3]),
        [x0] "=&s"(mx0), [x1] "=&s"(mx1), [cnt] "+s"(cnt)
      : [vo] "v"(vo), [rs] "s"(rdesc), [kx] "s"(KX)
      : "memory", "scc");
  }
  if (cnt == 0xffffffffu) return;
  float gl[4];
#pragma unroll
  for (int i = 0; i < 4; ++i) gl[i] = __uint_as_float(gr[i][1] ^ hA);
  float M = fmaxf(fmaxf(gl[0], gl[1]), fmaxf(gl[2], gl[3]));
  M = xg_max(fmaxf(M, dpp<0x128>(M)));
  const float Ms = (M == -INFINITY) ? 0.f : M;
  float W = 0.f, a0 = 0.f, a1 = 0.f, a2 = 0.f, a3 = 0.f;
#pragma unroll
  for (int i = 0; i < 4; ++i) {
    const float e = __builtin_amdgcn_exp2f(gl[i] - Ms);
    W += e;
    a0 = fmaf(e, __uint_as_float(gr[i][0] << 16), a0); a1 = fmaf(e, __uint_as_float(gr[i][0] & 0xffff0000u), a1);
    a2 = fmaf(e, __uint_as_float(gr[i][2] << 16), a2); a3 = fmaf(e, __uint_as_float(gr[i][2] & 0xffff0000u), a3);
  }
  const auto s01 = __builtin_amdgcn_permlane32_swap(__float_as_uint(a0), __float_as_uint(a1), false, false);
  const auto s23 = __builtin_amdgcn_permlane32_swap(__float_as_uint(a2), __float_as_uint(a3), false, false);
  const float u01 = __uint_as_float(s01[0]) + __uint_as_float(s01[1]);
  const float u23 = __uint_as_float(s23[0]) + __uint_as_float(s23[1]);
  const auto sx = __builtin_amdgcn_permlane16_swap(__float_as_uint(u01), __float_as_uint(u23), false, false);
  float v = __uint_as_float(sx[0]) + __uint_as_float(sx[1]);
  v += dpp<0x128>(v);
  W = xg_sum(W + dpp<0x128>(W));
  float* slot = lds + LDS_FIN + w * FIN_ROW;
  unsigned tk = 0;
  if (n < 8) {
    slot[4 * n + 2 * (g & 1) + (g >> 1)] = v;
    if (lane == 0) {
      *(f32x2*)(slot + 32) = f32x2{M, W};
      CB();
      int ci = 5;
      asm volatile("" : "+v"(ci));
      tk = LDS_ADD(ctl + ci, 1u);
    }
  }
  CB();
  const float* fin = lds + LDS_FIN;
  const int dd = lane & 31;
  float mw[8], ww[8], av[8];
#pragma unroll
  for (int i = 0; i < 8; ++i) {
    const f32x2 x = *(const f32x2*)(fin + i * FIN_ROW + 32);
    mw[i] = x[0]; ww[i] = x[1]; av[i] = fin[i * FIN_ROW + dd];
  }
  CB();
  if (__builtin_amdgcn_readfirstlane(tk) != 7u) return;
  M = fmaxf(fmaxf(fmaxf(mw[0], mw[1]), fmaxf(mw[2], mw[3])), fmaxf(fmaxf(mw[4], mw[5]), fmaxf(mw[6], mw[7])));
  const float Mf = (M == -INFINITY) ? 0.f : M;
  float Wt = 0.f, at = 0.f;
#pragma unroll
  for (int i = 0; i < 8; ++i) {
    const float fi = __builtin_amdgcn_exp2f(mw[i] - Mf);
    Wt = fmaf(fi, ww[i], Wt); at = fmaf(fi, av[i], at);
  }
  if (__builtin_expect(ctx < LK && !(M > -1.0e9f * LOG2E + 2048.f), 0)) {
    const float lm = -1.0e9f * LOG2E;
    const float Mall = fmaxf(M, lm);
    const float wv = (M == -INFINITY) ? 0.f : __builtin_amdgcn_exp2f(M - Mall);
    const float wm = __builtin_amdgcn_exp2f(lm - Mall);
    float sv = 0.f;
    const int hk = h >> 3;
    for (int t = ctx + (lane >> 5); t < LK; t += 2) {
      const unsigned short d = *(const unsigned short*)(vc + ((size_t)bt[t / BS] * BS + (t % BS)) * (HKV * D * 2) + hk * (D * 2) + (u * 32 + dd) * 2);
      sv += __uint_as_float((unsigned)d << 16);
    }
    sv += __shfl_xor(sv, 32);
    at = at * wv + sv * wm;
    Wt = Wt * wv + wm * (float)(LK - ctx);
  }
  const float res = at * __builtin_amdgcn_rcpf(Wt);
  const unsigned pk = cvtpk(res, dpp<0xB1>(res));
  const __amdgpu_buffer_rsrc_t ro = __builtin_amdgcn_make_buffer_rsrc((void*)out, 0, H * D * 2, 0x00020000);
  __builtin_amdgcn_raw_buffer_store_b32(pk, ro, (lane < 32 && !(lane & 1)) ? (h * D + u * 32 + lane) * 2 : OOB, 0, AUX_SC1);
  if (c == 0 && lane == 0) __hip_atomic_store((unsigned*)ws, e1_, __ATOMIC_RELAXED, __HIP_MEMORY_SCOPE_AGENT);
}

}

extern "C" void kernel_launch(void* const* d_in, const int* in_sizes, int n_in, void* d_out, int out_size, void* d_ws,
                              size_t ws_size, hipStream_t stream) {
  attn_decode<<<NWG, NTHR, 0, stream>>>((const char*)d_in[0], (const char*)d_in[1], (const char*)d_in[2], (const int*)d_in[3],
                                        (const int*)d_in[4], (char*)d_ws, (char*)d_out, in_sizes[1] * 2, 0);
}
```
